```python
import torch

def _softmax(x, axis=-1):
    return torch.softmax(x, dim=axis)
def _gelu(x):
    return torch.nn.functional.gelu(x, approximate="tanh")
def _silu(x):
    return torch.nn.functional.silu(x)
def _sigmoid(x):
    return torch.sigmoid(x)
def _layernorm(x, axis=-1, eps=1e-5):
    m = x.mean(dim=axis, keepdim=True)
    v = x.var(dim=axis, keepdim=True, unbiased=False)
    return (x - m) / torch.sqrt(v + eps)
def _scatter_add(tgt, idx, src, axis=0):
    return tgt.clone().index_add_(axis, idx, src)
def _segment_sum(src, seg, n):
    out = torch.zeros((n,) + src.shape[1:], dtype=src.dtype)
    return out.index_add_(0, seg, src)
def _pad(x, pw):
    flat = tuple(v for pair in reversed(pw) for v in pair)
    return torch.nn.functional.pad(x, flat)

kernel_name = 'gated_mix__ssm_conv_s4__ssm_conv_s4d__prompt'

def setup_inputs():

    _w = (lambda a: (lambda t: t.to(torch.bfloat16) if t.is_floating_point() else t)(torch.as_tensor(a)))
    def _A_setup():
        B, L, D, N = 1, 2048, 640, 192
        A_re = -(lambda _x: torch.exp(torch.as_tensor(_x)))((lambda *s: torch.randn(*s, dtype=torch.bfloat16) * 6.4)(D, N))
        A_im = torch.pi * (lambda *s: torch.randn(*s, dtype=torch.bfloat16) * 0.05)(D, N)
        B_re = (lambda *s: torch.randn(*s, dtype=torch.bfloat16) * 0.05)(D, N)
        B_im = (lambda *s: torch.randn(*s, dtype=torch.bfloat16) * 0.05)(D, N)
        C_re = (lambda *s: torch.randn(*s, dtype=torch.bfloat16) * 0.05)(D, N)
        C_im = (lambda *s: torch.randn(*s, dtype=torch.bfloat16) * 0.05)(D, N)
        log_dt = (lambda *s: torch.randn(*s, dtype=torch.bfloat16) * 0.05)(D) * 0.5 - 4.0
        Dskip = (lambda *s: torch.randn(*s, dtype=torch.bfloat16) * 0.05)(D)
        return {
            "u": _w((lambda *s: torch.randn(*s, dtype=torch.bfloat16) * 0.05)(B, L, D)),
            "A_re": _w(A_re),
            "A_im": _w(A_im),
            "B_re": _w(B_re),
            "B_im": _w(B_im),
            "C_re": _w(C_re),
            "C_im": _w(C_im),
            "log_dt": _w(log_dt),
            "Dskip": _w(Dskip),
        }
    _A_in = _A_setup()

    def _B_setup():
        Bsz, L, D, N = 1, 2048, 640, 192
        return {
            "u": _w((lambda *s: torch.randn(*s, dtype=torch.bfloat16) * 0.05)(Bsz, L, D)),
            "log_A_real": _w((lambda *s: torch.randn(*s, dtype=torch.bfloat16) * 6.4)(D, N) * 0.5),
            "A_imag": _w((lambda *s: torch.randn(*s, dtype=torch.bfloat16) * 0.05)(D, N) * 3.14),
            "C_real": _w((lambda *s: torch.randn(*s, dtype=torch.bfloat16) * 0.05)(D, N) / N ** 0.5),
            "C_imag": _w((lambda *s: torch.randn(*s, dtype=torch.bfloat16) * 0.05)(D, N) / N ** 0.5),
            "log_dt": _w((lambda *s: torch.randn(*s, dtype=torch.bfloat16) * 0.05)(D) * 0.5 - 4.0),
            "D_skip": _w((lambda *s: torch.randn(*s, dtype=torch.bfloat16) * 0.05)(D)),
        }
    _B_in = _B_setup()

    return {
        "A_u": _A_in["u"],
        "A_A_re": _A_in["A_re"],
        "A_A_im": _A_in["A_im"],
        "A_B_re": _A_in["B_re"],
        "A_B_im": _A_in["B_im"],
        "A_C_re": _A_in["C_re"],
        "A_C_im": _A_in["C_im"],
        "A_log_dt": _A_in["log_dt"],
        "A_Dskip": _A_in["Dskip"],
        "B_u": _B_in["u"],
        "B_log_A_real": _B_in["log_A_real"],
        "B_A_imag": _B_in["A_imag"],
        "B_C_real": _B_in["C_real"],
        "B_C_imag": _B_in["C_imag"],
        "B_log_dt": _B_in["log_dt"],
        "B_D_skip": _B_in["D_skip"],
        "gate_w": _w((lambda *s: torch.randn(*s, dtype=torch.bfloat16) * 0.05)(640) * 16.0),
    }

def reference(A_u, A_A_re, A_A_im, A_B_re, A_B_im, A_C_re, A_C_im, A_log_dt, A_Dskip, B_u, B_log_A_real, B_A_imag, B_C_real, B_C_imag, B_log_dt, B_D_skip, gate_w):
    def _A_ref(u, A_re, A_im, B_re, B_im, C_re, C_im, log_dt, Dskip):
        Bsz, L, D = u.shape
        dt = torch.exp(log_dt)[:, None]
        A = A_re + 1j * A_im
        Bc = B_re + 1j * B_im
        Cc = C_re + 1j * C_im
        logdA = dt * A
        dB = dt * Bc
        l = (lambda x, dt: x.to(dt))(torch.arange(L), A_re.dtype)
        P = torch.exp(logdA[:, :, None] * l[None, None, :])
        K = torch.einsum("dn,dnl,dn->dl", Cc, P, dB).real
        L2 = 2 * L
        Kp = _pad(K, ((0, 0), (0, L)))
        up = (lambda x, *d: x.permute(*d))(u, 0, 2, 1)
        up = _pad(up, ((0, 0), (0, 0), (0, L)))
        Kf = (lambda x, axes=(-2,-1): torch.fft.rfft2(x.float(), dim=axes))(Kp[:, None, :], axes=(-2, -1))
        Uf = (lambda x, axes=(-2,-1): torch.fft.rfft2(x.float(), dim=axes))(up[:, :, None, :], axes=(-2, -1))
        Yf = Uf * Kf[None, :, :, :]
        y = (lambda x, s, axes=(-2,-1): torch.fft.irfft2(x, s=s, dim=axes).to(torch.bfloat16))(Yf, (1, L2), axes=(-2, -1))[:, :, 0, :L]
        y = (lambda x, *d: x.permute(*d))(y, 0, 2, 1) + u * Dskip[None, None, :]
        return y

    def _B_ref(u, log_A_real, A_imag, C_real, C_imag, log_dt, D_skip):
        L = u.shape[1]
        dt = torch.exp(log_dt)
        A = -torch.exp(log_A_real) + 1j * A_imag
        C = (C_real + 1j * C_imag) * dt[:, None]
        pos = (lambda x, dt: x.to(dt))(torch.arange(L), log_dt.dtype)
        P = torch.exp((A * dt[:, None])[:, :, None] * pos[None, None, :])
        K = 2.0 * torch.einsum("dn,dnl->dl", C, P).real
        L2 = 2 * L
        u_t = (lambda x, *d: x.permute(*d))(u, 0, 2, 1)
        Kf = (lambda x, *a, **k: torch.fft.rfft(x.float(), *a, **k))(K, L2)
        Uf = (lambda x, *a, **k: torch.fft.rfft(x.float(), *a, **k))(u_t, L2)
        y = (lambda x, *a, **k: torch.fft.irfft(x, *a, **k).to(torch.bfloat16))(Uf * Kf[None, :, :], L2)[:, :, :L]
        y = (lambda x, *d: x.permute(*d))(y, 0, 2, 1)
        return y + u * D_skip[None, None, :]

    x = A_u
    g = _sigmoid((x * gate_w).sum(-1, keepdims=True))
    a = _A_ref(x, A_A_re, A_A_im, A_B_re, A_B_im, A_C_re, A_C_im, A_log_dt, A_Dskip)
    b = _B_ref(x, B_log_A_real, B_A_imag, B_C_real, B_C_imag, B_log_dt, B_D_skip)
    return g * a + (1 - g) * b
```

```cpp
#include <hip/hip_runtime.h>
#include <hip/hip_bf16.h>
#include <stdint.h>

constexpr int L = 2048, D = 640, N = 192;

__device__ __forceinline__ float bf2f(uint16_t v) { return __uint_as_float(((uint32_t)v) << 16); }
__device__ __forceinline__ float rbf(float x) { return __bfloat162float(__float2bfloat16(x)); }

__global__ void kgen(const uint16_t* Are, const uint16_t* Aim, const uint16_t* Bre, const uint16_t* Bim,
                     const uint16_t* Cre, const uint16_t* Cim, const uint16_t* ldtA,
                     const uint16_t* lAr, const uint16_t* Aimag, const uint16_t* Cr, const uint16_t* Ci,
                     const uint16_t* ldtB, float* K) {
  int d = blockIdx.x, br = blockIdx.y;
  __shared__ float zr[N], zi[N], wr[N], wi[N];
  int t = threadIdx.x;
  if (t < N) {
    int i = d * N + t;
    if (br == 0) {
      float dt = rbf(expf(bf2f(ldtA[d])));
      zr[t] = dt * bf2f(Are[i]); zi[t] = dt * bf2f(Aim[i]);
      float br_ = dt * bf2f(Bre[i]), bi_ = dt * bf2f(Bim[i]);
      float cr = bf2f(Cre[i]), ci = bf2f(Cim[i]);
      wr[t] = cr * br_ - ci * bi_; wi[t] = cr * bi_ + ci * br_;
    } else {
      float dt = rbf(expf(bf2f(ldtB[d])));
      zr[t] = -rbf(expf(bf2f(lAr[i]))) * dt; zi[t] = dt * bf2f(Aimag[i]);
      wr[t] = 2.f * dt * bf2f(Cr[i]); wi[t] = 2.f * dt * bf2f(Ci[i]);
    }
  }
  __syncthreads();
  for (int l = t; l < L; l += blockDim.x) {
    float p = rbf((float)l);
    float acc = 0.f;
    for (int n = 0; n < N; ++n) {
      float e = expf(zr[n] * p);
      float s, c; sincosf(zi[n] * p, &s, &c);
      acc += e * (wr[n] * c - wi[n] * s);
    }
    K[((size_t)br * D + d) * L + l] = acc;
  }
}

__global__ void gate(const uint16_t* u, const uint16_t* gw, float* g) {
  int l = blockIdx.x;
  float acc = 0.f;
  for (int d = threadIdx.x; d < D; d += 64) acc += rbf(bf2f(u[l * D + d]) * bf2f(gw[d]));
  for (int o = 32; o; o >>= 1) acc += __shfl_xor(acc, o);
  if (threadIdx.x == 0) { float s = rbf(acc); g[l] = rbf(1.f / (1.f + expf(-s))); }
}

__global__ void conv(const uint16_t* u, const float* K, const float* g, const uint16_t* DA, const uint16_t* DB,
                     __hip_bfloat16* out) {
  int d = blockIdx.x;
  __shared__ float su[L], ka[L], kb[L];
  for (int l = threadIdx.x; l < L; l += blockDim.x) {
    su[l] = bf2f(u[l * D + d]); ka[l] = K[(size_t)d * L + l]; kb[l] = K[((size_t)D + d) * L + l];
  }
  __syncthreads();
  float da = bf2f(DA[d]), db = bf2f(DB[d]);
  for (int l = threadIdx.x; l < L; l += blockDim.x) {
    float ya = 0.f, yb = 0.f;
    for (int j = 0; j <= l; ++j) { float x = su[l - j]; ya += ka[j] * x; yb += kb[j] * x; }
    float a = rbf(rbf(ya) + rbf(su[l] * da));
    float b = rbf(rbf(yb) + rbf(su[l] * db));
    float gg = g[l];
    out[l * D + d] = __float2bfloat16(rbf(gg * a) + rbf(rbf(1.f - gg) * b));
  }
}

extern "C" void kernel_launch(void* const* d_in, const int* in_sizes, int n_in,
                              void* d_out, int out_size, void* d_ws, size_t ws_size,
                              hipStream_t stream) {
  const uint16_t* const* in = (const uint16_t* const*)d_in;
  float* K = (float*)d_ws;
  float* g = K + 2 * D * L;
  kgen<<<dim3(D, 2), 256, 0, stream>>>(in[1], in[2], in[3], in[4], in[5], in[6], in[7], in[10], in[11], in[12], in[13], in[14], K);
  gate<<<L, 64, 0, stream>>>(in[0], in[16], g);
  conv<<<D, 256, 0, stream>>>(in[0], K, g, in[8], in[15], (__hip_bfloat16*)d_out);
}
```

```cpp
#include <hip/hip_runtime.h>
#include <hip/hip_bf16.h>
#include <stdint.h>
#pragma clang diagnostic ignored "-Wint-to-pointer-cast"

typedef unsigned long long u64;
typedef unsigned u32;
typedef unsigned short u16;
typedef u32 u32x2 __attribute__((ext_vector_type(2)));
typedef u32 u32x4 __attribute__((ext_vector_type(4)));
typedef short s16x4 __attribute__((ext_vector_type(4)));
typedef float f32x2 __attribute__((ext_vector_type(2)));
typedef float f32x4 __attribute__((ext_vector_type(4)));
typedef float f32x16 __attribute__((ext_vector_type(16)));
typedef _Float16 h16x2 __attribute__((ext_vector_type(2)));
typedef _Float16 h16x8 __attribute__((ext_vector_type(8)));
typedef __bf16 b16x2 __attribute__((ext_vector_type(2)));
typedef __bf16 b16x8 __attribute__((ext_vector_type(8)));
#define LAS __attribute__((address_space(3)))
#define GAS __attribute__((address_space(1)))
#define RLX_AGENT __ATOMIC_RELAXED, __HIP_MEMORY_SCOPE_AGENT
#define DEV __device__ __forceinline__

constexpr int L = 2048, D = 640, N = 192;
constexpr int U_OFF = 0, U_CH = 17312, U_R = 4320;
constexpr int W3_OFF = 69248, W3_CH = 3744;
constexpr int W5_OFF = 84224, W5_CH = 3232;
constexpr int W7_OFF = 97152, W7_CH = 2208;
constexpr int O_OFF = 105984;
constexpr int T_ROW = 2064;
constexpr int R_OFF = 122368;
constexpr int KR_OFF = 138816, KR_ST = 896;
constexpr int KS_OFF = 145984, KS_ST = 1088;
constexpr int G_OFF = 154688;
constexpr int MISC_OFF = 162880;
constexpr int LDS_BYTES = 163840;
constexpr int WS_ACK = 4096, WS_GR = 8192, WS_STAMP = 1 << 20;

DEV float bf_lo(u32 v) { return __uint_as_float(v << 16); }
DEV float bf_hi(u32 v) { return __uint_as_float(v & 0xffff0000u); }
DEV u32 pk_bf(float a, float b) { f32x2 x = {a, b}; b16x2 y = __builtin_convertvector(x, b16x2); return __builtin_bit_cast(u32, y); }
DEV float rbf(float a) { return bf_lo(pk_bf(a, a)); }
DEV h16x2 pk_h(f32x2 x) { return __builtin_convertvector(x, h16x2); }
DEV h16x2 cmul_h(h16x2 x, h16x2 m) {
  h16x2 t = x * (h16x2){m[0], m[0]};
  return __builtin_elementwise_fma((h16x2){-x[1], x[0]}, (h16x2){m[1], m[1]}, t);
}
DEV h16x2 cmulc_h(h16x2 x, h16x2 m) {
  h16x2 t = x * (h16x2){m[0], m[0]};
  return __builtin_elementwise_fma((h16x2){x[1], -x[0]}, (h16x2){m[1], m[1]}, t);
}
DEV f32x2 cmulc_f(f32x2 x, f32x2 m) {
  f32x2 t = x * (f32x2){m[0], m[0]};
  return __builtin_elementwise_fma((f32x2){x[1], -x[0]}, (f32x2){m[1], m[1]}, t);
}
DEV f32x2 csq_f(f32x2 x) {
  f32x2 t = x * (f32x2){x[0], x[0]};
  return __builtin_elementwise_fma((f32x2){-x[1], x[0]}, (f32x2){x[1], x[1]}, t);
}
template <int OFF> DEV void tw(h16x2 v) {
  asm volatile("ds_write_addtid_b32 %0 offset:%1" ::"v"(v), "i"(OFF) : "memory");
}
DEV void set_m0(u32 v) { asm volatile("s_mov_b32 m0, %0\n\ts_nop 0" ::"s"(v) : "memory"); }
template <typename T> DEV T lds_ld(u32 a) { return *(LAS T*)a; }
template <typename T> DEV void lds_st(u32 a, T v) { *(LAS T*)a = v; }
DEV u32x2 lds_tr(u32 a) { return __builtin_bit_cast(u32x2, __builtin_amdgcn_ds_read_tr16_b64_v4i16((LAS s16x4*)a)); }
DEV u64 next_tag(u64 e) { return e * 6364136223846793005ull + 1442695040888963407ull; }
DEV void wg_barrier() { asm volatile("s_waitcnt lgkmcnt(0)\n\ts_barrier" ::: "memory"); }
DEV f32x4 mm(u32x4 a, u32x4 b, f32x4 c) {
  return __builtin_amdgcn_mfma_f32_16x16x32_bf16(__builtin_bit_cast(b16x8, a), __builtin_bit_cast(b16x8, b), c, 0, 0, 0);
}
DEV u32x4 cat(u32x2 a, u32x2 b) { return (u32x4){a.x, a.y, b.x, b.y}; }
#define ST(i)

template <bool DO_U, int NW> DEV void prep_block(int gam, const u32 (&ra)[4], u32 wu, u32 ww) {
  u32 d[8];
#pragma unroll
  for (int n = 0; n < 4; ++n) { u32x2 v = lds_tr(ra[n] + 1024 * gam); d[2 * n] = v.x; d[2 * n + 1] = v.y; }
  if (DO_U) {
    u32 o[5];
#pragma unroll
    for (int k = 0; k < 5; ++k) o[k] = __builtin_amdgcn_alignbit(d[k + 3], d[k + 2], 16);
    u32 a = wu + 256 * gam;
    lds_st<u32x4>(a, (u32x4){d[4], d[5], d[6], d[7]});
    lds_st<u32x4>(a + U_R, (u32x4){o[1], o[2], o[3], o[4]});
    lds_st<u32x4>(a + 2 * U_R, (u32x4){d[3], d[4], d[5], d[6]});
    lds_st<u32x4>(a + 3 * U_R, (u32x4){o[0], o[1], o[2], o[3]});
  }
  if (NW > 0) {
    float P[16];
    constexpr int k0 = 4 - NW, k1 = 11 + NW;
    P[k0 - 1] = 0.f;
#pragma unroll
    for (int k = k0; k <= k1; ++k) { float v = (k & 1) ? bf_hi(d[k >> 1]) : bf_lo(d[k >> 1]); P[k] = k == k0 ? v : P[k - 1] + v; }
#pragma unroll
    for (int h = 1; h <= NW; ++h) {
      u32 r[4];
#pragma unroll
      for (int j = 0; j < 8; j += 2) r[j >> 1] = pk_bf(P[4 + j + h] - P[3 + j - h], P[5 + j + h] - P[4 + j - h]);
      u32 a = (h == 1 ? W3_OFF + (ww >> 16) * W3_CH : h == 2 ? W5_OFF + (ww >> 16) * W5_CH : W7_OFF + (ww >> 16) * W7_CH) + (ww & 0xffff) + 256 * gam;
      lds_st<u32x4>(a, (u32x4){r[0], r[1], r[2], r[3]});
    }
  }
}

__global__ void __launch_bounds__(512, 2)
ssm_mix(const u16* __restrict__ u, const u16* __restrict__ aAre, const u16* __restrict__ aAim,
        const u16* __restrict__ aBre, const u16* __restrict__ aBim, const u16* __restrict__ aCre,
        const u16* __restrict__ aCim, const u16* __restrict__ aLdt, const u16* __restrict__ aD,
        const u16* __restrict__ bLA, const u16* __restrict__ bAim, const u16* __restrict__ bCre,
        const u16* __restrict__ bCim, const u16* __restrict__ bLdt, const u16* __restrict__ bD,
        const u16* __restrict__ gw, u16* __restrict__ out, char* __restrict__ ws) {
  extern __shared__ __attribute__((aligned(16))) char smem[];
  asm volatile(".Lssm_code_0:");
  const int tid = threadIdx.x, lane = tid & 63;
  const int w = __builtin_amdgcn_readfirstlane(tid >> 6);
  const int bx = blockIdx.x & 7, bj = blockIdx.x >> 3;
  GAS u64* wsE = (GAS u64*)ws;
  GAS u64* wsAck = (GAS u64*)(ws + WS_ACK);
  GAS u64* wsGr = (GAS u64*)(ws + WS_GR);
  ST(0);
  if (bj == 0 || bj > 20) {
    if (bj == 0) {
      if (w < 7) {
        u64 pc; int back, len;
        asm volatile("s_getpc_b64 %0\n.Lssm_code_a:\n\ts_mov_b32 %1, .Lssm_code_a-.Lssm_code_0\n\ts_mov_b32 %2, .Lssm_code_b-.Lssm_code_0"
                     : "=s"(pc), "=s"(back), "=s"(len));
        if (tid * 128 < len) {
          u32 v = __builtin_nontemporal_load((const GAS u32*)(pc - back) + tid * 32);
          asm volatile("" ::"v"(v));
        }
        return;
      }
      u64 T = next_tag(__hip_atomic_load(wsE, RLX_AGENT));
      if (lane == 0) __hip_atomic_store(wsAck + blockIdx.x, T, RLX_AGENT);
      if (bx != 0) return;
      for (int spin = 0; spin < 200000; ++spin) {
        __builtin_amdgcn_s_sleep(16);
        bool ok = true;
#pragma unroll
        for (int i = 0; i < 4; ++i) ok &= __hip_atomic_load(wsAck + i * 64 + lane, RLX_AGENT) == T;
        if (__all(ok)) { if (lane == 0) __hip_atomic_store(wsE, T, RLX_AGENT); break; }
      }
      return;
    }
    const int hh = (bj - 21) * 8 + bx;
    float s = 0.f;
    const int row = hh * 24 + w * 4 + (lane >> 4);
    if (w < 6 && hh * 24 + w * 4 < L) {
      const u32x4* pu = (const u32x4*)(u + (size_t)row * D) + (lane & 15);
      const u32x4* pw = (const u32x4*)gw + (lane & 15);
      u32x4 a[5], b[5];
#pragma unroll
      for (int i = 0; i < 5; ++i) { a[i] = pu[16 * i]; b[i] = pw[16 * i]; }
#pragma unroll
      for (int i = 0; i < 5; ++i)
#pragma unroll
        for (int k = 0; k < 4; ++k) {
          const u32 x = a[i][k], y = b[i][k];
          s = __builtin_amdgcn_fdot2_f32_bf16(__builtin_bit_cast(b16x2, x), __builtin_bit_cast(b16x2, y), s, false);
        }
      s += __shfl_xor(s, 1); s += __shfl_xor(s, 2); s += __shfl_xor(s, 4); s += __shfl_xor(s, 8);
    } else if (w == 7) {
      u64 T = next_tag(__hip_atomic_load(wsE, RLX_AGENT));
      if (lane == 0) { __hip_atomic_store(wsAck + blockIdx.x, T, RLX_AGENT); lds_st<u64>(MISC_OFF, T); }
    }
    wg_barrier();
    if (w < 6 && hh * 24 + w * 4 < L && (lane & 15) == 0) {
      u64 T = lds_ld<u64>(MISC_OFF);
      s = rbf(s);
      float g = __builtin_amdgcn_rcpf(1.f + __builtin_amdgcn_exp2f(-1.4426950408889634f * s));
      __hip_atomic_store(wsGr + row, (T & ~0xffffull) | (pk_bf(g, g) & 0xffffu), RLX_AGENT);
    }
    ST(1);
    return;
  }
  const int c0 = 80 * bx + 4 * (bj - 1);
  const int ch = w & 3, br = w >> 2, d = c0 + ch;
  u32 p2[6]; u32 p1[6];
  {
    const int o2 = d * N + 2 * lane, o1 = d * N + 128 + lane;
    if (br == 0) {
      const u16* a[6] = {aAre, aAim, aCre, aCim, aBre, aBim};
#pragma unroll
      for (int i = 0; i < 6; ++i) { p2[i] = *(const u32*)(a[i] + o2); p1[i] = a[i][o1]; }
    } else {
      const u16* a[4] = {bLA, bAim, bCre, bCim};
#pragma unroll
      for (int i = 0; i < 4; ++i) { p2[i] = *(const u32*)(a[i] + o2); p1[i] = a[i][o1]; }
      p2[4] = p2[5] = p1[4] = p1[5] = 0;
    }
  }
  const float ldt = bf_lo((br ? bLdt : aLdt)[d]);
  const float dsk = bf_lo((br ? bD : aD)[d]);
  u64 E = 0;
  if (w == 0) E = __hip_atomic_load(wsE, RLX_AGENT);
  u32x2 ur[4];
#pragma unroll
  for (int i = 0; i < 4; ++i) ur[i] = *(const u32x2*)(u + (size_t)(256 * w + 64 * i + lane) * D + c0);
  const u32 krb = KR_OFF + (2 * ch + br) * KR_ST, ksb = KS_OFF + (2 * ch + br) * KS_ST;
  {
    const u32x4 z = {0, 0, 0, 0};
    if (lane < 56) lds_st<u32x4>(krb + 16 * lane, z);
    lds_st<u32x4>(ksb + 16 * lane, z);
    if (lane < 4) lds_st<u32x4>(ksb + 1024 + 16 * lane, z);
    if (w == 0 && lane < 4) lds_st<u32x4>(R_OFF + 16 * lane, z);
    lds_st<u32>(21 * T_ROW + 256 * w + 4 * lane, 0x00003c00u);
  }
  f32x2 zz[3], ww[3];
  float unscale;
  {
    const float dt = rbf(__builtin_amdgcn_exp2f(1.4426950408889634f * ldt));
    const f32x2 dz = {dt * 1.4426950408889634f, dt * 0.15915494309189535f};
    int emax = -120;
#pragma unroll
    for (int k = 0; k < 3; ++k) {
      float v[6];
#pragma unroll
      for (int i = 0; i < 6; ++i) v[i] = k == 0 ? bf_lo(p2[i]) : k == 1 ? bf_hi(p2[i]) : bf_lo(p1[i]);
      f32x2 cw;
      if (br == 0) {
        cw = (f32x2){v[2] * v[4] - v[3] * v[5], -(v[2] * v[5] + v[3] * v[4])} * dt;
      } else {
        v[0] = -rbf(__builtin_amdgcn_exp2f(1.4426950408889634f * v[0]));
        cw = (f32x2){v[2], -v[3]} * (2.f * dt);
      }
      zz[k] = (f32x2){v[0], v[1]} * dz;
      ww[k] = cw;
      int e = __builtin_amdgcn_frexp_expf(fmaxf(fabsf(cw[0]), fabsf(cw[1])));
      if (br == 0) e += (int)fminf(fmaxf(ceilf(2048.f * zz[k][0]), 0.f), 200.f);
      emax = max(emax, e);
    }
#pragma unroll
    for (int o = 1; o < 64; o <<= 1) emax = max(emax, __shfl_xor(emax, o));
    emax = __builtin_amdgcn_readfirstlane(min(emax, 126));
    const float sc = __int_as_float((127 + 14 - emax > 254 ? 254 : 127 + 14 - emax) << 23);
    unscale = 1.f / sc;
#pragma unroll
    for (int k = 0; k < 3; ++k) ww[k] *= sc;
  }
  if (w == 0) {
    const u64 T = next_tag(E);
    if (lane == 0) { __hip_atomic_store(wsAck + blockIdx.x, T, RLX_AGENT); lds_st<u64>(MISC_OFF, T); }
  }
  ST(1);
  f32x16 acc = {0};
  u32x4 gr0, gr1;
  const u32 fa = (lane & 31) * T_ROW + 256 * w + 16 * (lane >> 5);
#pragma unroll 1
  for (int k = 0; k < 3; ++k) {
    const f32x2 z = zz[0], W = ww[0];
    zz[0] = zz[1]; zz[1] = zz[2]; ww[0] = ww[1]; ww[1] = ww[2];
    const float e = __builtin_amdgcn_exp2f(z[0]), f = __builtin_amdgcn_fractf(z[1]);
    f32x2 m = (f32x2){__builtin_amdgcn_cosf(f), __builtin_amdgcn_sinf(f)} * e;
    h16x2 M[8];
#pragma unroll
    for (int i = 0; i < 8; ++i) { M[i] = pk_h(m); m = csq_f(m); }
    set_m0(256 * w);
    {
      h16x2 Y[8];
      Y[0] = pk_h(W); tw<0>(Y[0]);
      Y[1] = cmulc_h(Y[0], M[0]); tw<1 * T_ROW>(Y[1]);
      Y[2] = cmulc_h(Y[0], M[1]); tw<2 * T_ROW>(Y[2]);
      Y[3] = cmulc_h(Y[1], M[1]); tw<3 * T_ROW>(Y[3]);
      Y[4] = cmulc_h(Y[0], M[2]); tw<4 * T_ROW>(Y[4]);
      Y[5] = cmulc_h(Y[1], M[2]); tw<5 * T_ROW>(Y[5]);
      Y[6] = cmulc_h(Y[2], M[2]); tw<6 * T_ROW>(Y[6]);
      Y[7] = cmulc_h(Y[3], M[2]); tw<7 * T_ROW>(Y[7]);
      f32x2 Z = cmulc_f(W, m);
      h16x2 a = pk_h(Z); tw<8 * T_ROW>(a);
      tw<9 * T_ROW>(cmulc_h(a, M[2]));
      a = cmulc_h(a, M[1]); tw<10 * T_ROW>(a);
      tw<11 * T_ROW>(cmulc_h(a, M[2]));
      Z = cmulc_f(Z, m); a = pk_h(Z); tw<12 * T_ROW>(a);
      tw<13 * T_ROW>(cmulc_h(a, M[2]));
      Z = cmulc_f(Z, m); a = pk_h(Z); tw<14 * T_ROW>(a);
      tw<15 * T_ROW>(cmulc_h(a, M[2]));
      Z = cmulc_f(Z, m); tw<16 * T_ROW>(pk_h(Z));
      Z = cmulc_f(Z, m); tw<17 * T_ROW>(pk_h(Z));
      Z = cmulc_f(Z, m); tw<18 * T_ROW>(pk_h(Z));
      Z = cmulc_f(Z, m); tw<19 * T_ROW>(pk_h(Z));
      Z = cmulc_f(Z, m); tw<20 * T_ROW>(pk_h(Z));
    }
    set_m0(22 * T_ROW + 256 * w);
    {
      h16x2 X[16];
#define COL(c, v) tw<((c) - 1) * T_ROW>(v)
      X[1] = M[3]; COL(1, X[1]);
      X[2] = M[4]; COL(2, X[2]);
      X[3] = cmul_h(X[1], M[4]); COL(3, X[3]);
      X[4] = M[5]; COL(4, X[4]);
      X[5] = cmul_h(X[1], M[5]); COL(5, X[5]);
      X[6] = cmul_h(X[2], M[5]); COL(6, X[6]);
      X[7] = cmul_h(X[3], M[5]); COL(7, X[7]);
      X[8] = M[6]; COL(8, X[8]);
#define C8(c) X[c] = cmul_h(X[(c) - 8], M[6]); COL(c, X[c]);
      C8(9) C8(10) C8(11) C8(12) C8(13) C8(14) C8(15)
      COL(16, M[7]);
#define C16(c) COL(c, cmul_h(X[(c) - 16], M[7]));
      C16(17) C16(18) C16(19) C16(20) C16(21) C16(22) C16(23) C16(24) C16(25) C16(26) C16(27) C16(28) C16(29) C16(30) C16(31)
    }
    if (k == 1) {
      auto rg = __builtin_amdgcn_make_buffer_rsrc((void*)(ws + WS_GR), 0, L * 8, 0x00020000);
      gr0 = __builtin_amdgcn_raw_buffer_load_b128(rg, tid * 16, 0, 16);
      gr1 = __builtin_amdgcn_raw_buffer_load_b128(rg, tid * 16 + 8192, 0, 16);
    }
#pragma unroll
    for (int s = 0; s < 8; ++s)
      acc = __builtin_amdgcn_mfma_f32_32x32x16_f16(lds_ld<h16x8>(fa + 32 * s), lds_ld<h16x8>(fa + 21 * T_ROW + 32 * s), acc, 0, 0, 0);
    asm volatile("" ::: "memory");
  }
  ST(2);
  {
    const int c = lane & 31, h = lane >> 5;
    float k[12];
#pragma unroll
    for (int i = 0; i < 12; ++i) k[i] = acc[i] * unscale;
    if (lane == 0) k[0] += dsk;
    lds_st<u32x2>(krb + 2 * (316 - 8 * c - 4 * h), (u32x2){pk_bf(k[3], k[2]), pk_bf(k[1], k[0])});
    const u32 x0 = pk_bf(k[4], k[5]), x1 = pk_bf(k[6], k[7]), y0 = pk_bf(k[8], k[9]), y1 = pk_bf(k[10], k[11]);
    if (h == 0) {
      if (c > 0) {
        lds_st<u32>(ksb + 2 * (2 * c + 12), x0);
        lds_st<u32>(ksb + 192 + 2 * (2 * c + 12), x1);
      } else {
        lds_st<u32x4>(krb + 2 * 56, (u32x4){x1 & 0xffff0000u, (x0 >> 16) * 0x10001u, (x0 >> 16) | (x1 << 16), (x0 & 0xffffu) * 0x10001u});
        lds_st<u16>(ksb + 384 + 4 * 70 + 2, (u16)y0);
      }
      lds_st<u16>(ksb + 736 + 2 * (8 + c), (u16)y0);
      lds_st<u16>(ksb + 736 + 2 * (40 + c), (u16)(y0 >> 16));
      lds_st<u16>(ksb + 736 + 2 * (72 + c), (u16)y1);
      lds_st<u16>(ksb + 736 + 2 * (104 + c), (u16)(y1 >> 16));
    } else {
      if (c > 0) lds_st<u16>(ksb + 384 + 4 * (c + 6) + 2, (u16)x0);
      else { lds_st<u16>(ksb + 2 * 76, (u16)x0); lds_st<u16>(ksb + 192 + 2 * 76, (u16)x0); lds_st<u16>(ksb + 736 + 2 * 136, (u16)y0); }
      lds_st<u16>(ksb + 384 + 4 * (c + 7), (u16)(x0 >> 16));
      lds_st<u16>(ksb + 384 + 4 * (c + 38) + 2, (u16)x1);
      lds_st<u16>(ksb + 384 + 4 * (c + 39), (u16)(x1 >> 16));
    }
  }
  {
    const int y = 8 + 256 * w + lane;
    const u32 a = R_OFF + 8 * (y ^ (((y >> 5) & 1) << 2));
#pragma unroll
    for (int i = 0; i < 4; ++i) lds_st<u32x2>(a + 512 * i, ur[i]);
  }
  wg_barrier();
  ST(3);
  {
    const u64 T = lds_ld<u64>(MISC_OFF);
    const u32 thi = (u32)(T >> 32), tlo = (u32)T >> 16;
    auto rg = __builtin_amdgcn_make_buffer_rsrc((void*)(ws + WS_GR), 0, L * 8, 0x00020000);
    for (int spin = 0; spin < 200000; ++spin) {
      bool ok = gr0.y == thi && gr0.w == thi && gr1.y == thi && gr1.w == thi &&
                (gr0.x >> 16) == tlo && (gr0.z >> 16) == tlo && (gr1.x >> 16) == tlo && (gr1.z >> 16) == tlo;
      if (__all(ok)) break;
      asm volatile("" ::: "memory");
      gr0 = __builtin_amdgcn_raw_buffer_load_b128(rg, tid * 16, 0, 16);
      gr1 = __builtin_amdgcn_raw_buffer_load_b128(rg, tid * 16 + 8192, 0, 16);
    }
    const int t = 2 * tid;
    const u32 a = G_OFF + 512 * (t >> 7) + 8 * (16 * ((t >> 4) & 3) + 8 * ((t >> 6) & 1) + (t & 7)) + 4 * ((t >> 3) & 1);
    lds_st<u32>(a, gr0.x << 16); lds_st<u32>(a + 8, gr0.z << 16);
    lds_st<u32>(a + 4096, gr1.x << 16); lds_st<u32>(a + 4096 + 8, gr1.z << 16);
  }
  ST(4);
  {
    u32 ra[4];
    const int G = lane >> 4, q = (lane >> 2) & 3, p = lane & 3;
#pragma unroll
    for (int n = 0; n < 4; ++n) { const int y = 32 * G + 8 * p + q + 4 * n; ra[n] = R_OFF + 8 * (y ^ (((y >> 5) & 1) << 2)); }
    const u32 wu = U_OFF + p * U_CH + 160 + 16 * (lane >> 2);
    const u32 wwp = (p << 16) | (160 + 16 * (lane >> 2));
    prep_block<true, 3>(w, ra, wu, wwp);
    if (w < 4) prep_block<true, 2>(w + 8, ra, wu, wwp);
    else if (w < 6) prep_block<true, 1>(w + 8, ra, wu, wwp);
    else prep_block<true, 0>(w + 8, ra, wu, wwp);
    if (tid < 280) {
      const int a = tid / 10, i = tid % 10;
      const u32 base = a < 16 ? U_OFF + (a >> 2) * U_CH + (a & 3) * U_R
                     : a < 20 ? W3_OFF + (a - 16) * W3_CH : a < 24 ? W5_OFF + (a - 20) * W5_CH : W7_OFF + (a - 24) * W7_CH;
      lds_st<u32x4>(base + 16 * i, (u32x4){0, 0, 0, 0});
    }
  }
  wg_barrier();
  ST(5);
  if (w < 4) {
    const int g = lane >> 4, abr = lane & 1, beta = (lane & 15) >> 1;
    const int ap = (lane >> 3) & 1, b = lane & 7, q = (lane >> 2) & 3, p = lane & 3;
    const u32 kr = KR_OFF + (2 * ch + abr) * KR_ST, ks = KS_OFF + (2 * ch + abr) * KS_ST;
    u32x4 A0[10], A1[5], A2[5], A3[8];
#pragma unroll
    for (int s = 0; s < 10; ++s) A0[s] = lds_ld<u32x4>(kr + 2 * (368 - 8 * (g + beta)) - 64 * s);
#pragma unroll
    for (int s = 0; s < 5; ++s) {
      const u32 a = ks + 2 * (4 * g + 2 * beta) + 32 * s;
      A1[s] = (u32x4){lds_ld<u32>(a), lds_ld<u32>(a + 4), lds_ld<u32>(a + 192), lds_ld<u32>(a + 196)};
    }
#pragma unroll
    for (int s = 0; s < 5; ++s) {
      const u32 a = ks + 384 + 4 * (4 * g + beta) + 64 * s;
      const u32 e0 = lds_ld<u32>(a), e1 = lds_ld<u32>(a + 4), e2 = lds_ld<u32>(a + 8), e3 = lds_ld<u32>(a + 12);
      A2[s] = (u32x4){__builtin_amdgcn_perm(e1, e0, 0x05040100), __builtin_amdgcn_perm(e3, e2, 0x05040100),
                      __builtin_amdgcn_perm(e1, e0, 0x07060302), __builtin_amdgcn_perm(e3, e2, 0x07060302)};
    }
    {
      const u32 k1024 = lds_ld<u16>(ks + 736 + 16);
      const int qq = beta - 4 * (g - 2);
      if (g >= 2 && qq >= 0 && qq < 4) { if (qq < 2) A2[4].x = k1024 << (16 * qq); else A2[4].y = k1024 << (16 * (qq - 2)); }
    }
    {
      const int m0 = -7 + 4 * g + beta, odd = m0 & 1;
      const u32 st = odd ? 0x05040504u : 0x05040100u, sc = odd ? 0x07060504u : 0x05040302u;
#pragma unroll
      for (int s = 0; s < 8; ++s) {
        const u32 a = ks + 736 + 2 * (m0 - odd + 8) + 32 * s;
        const u32 e0 = lds_ld<u32>(a), e1 = lds_ld<u32>(a + 4), e2 = lds_ld<u32>(a + 8);
        A3[s] = (u32x4){__builtin_amdgcn_perm(e1, e0, st), __builtin_amdgcn_perm(e2, e1, st),
                        __builtin_amdgcn_perm(e1, e0, sc), __builtin_amdgcn_perm(e2, e1, sc)};
      }
      const int qq = -1 - m0;
      const u32 k0 = qq == 0 ? 0xffff0000u : qq == 1 ? 0x0000ffffu : ~0u, k1 = qq == 2 ? 0xffff0000u : qq == 3 ? 0x0000ffffu : ~0u;
      A3[0].x &= k0; A3[0].z &= k0; A3[0].y &= k1; A3[0].w &= k1;
    }
    const u32 uc = U_OFF + ch * U_CH;
    const u32 b0 = uc + (3 - (b & 3)) * U_R + 2 * (64 * ap + 4 * (b >> 2) + 132 - 8 * g - 96);
    const int xb = 64 * (p >> 1) + 4 * (p & 1);
    const u32 b1w = W3_OFF + ch * W3_CH + 2 * (xb - 208 - 16 * g - 4 * q + 84 + 192);
    const u32 b1u = uc + 2 * U_R + 2 * (xb - 208 - 16 * g - 4 * q + 80 + 192);
    const u32 b2a = W3_OFF + ch * W3_CH + 2 * (xb - 460 - 32 * g - 8 * q + 84 + 512);
    const u32 b2b = W5_OFF + ch * W5_CH + 2 * (xb - 460 - 32 * g - 8 * q + 80 + 512);
    const u32 b2s = g < 2 ? b2a : uc + 3 * U_R + 2 * (xb + 8 * (q + 4 * (g - 2)) - 944 + 1024);
    const u32 b3u = uc + 2 * (xb + 52 - 32 * g - 8 * q + 80);
    const u32 b3w = W7_OFF + ch * W7_CH + 2 * (xb + 52 - 32 * g - 8 * q + 80);
    set_m0(56832 + 256 * ch);
    u32x4 B0[64], B1[28], B2[12], B3[8];
#pragma unroll
    for (int al = 0; al < 16; ++al) {
      f32x4 a0 = {0, 0, 0, 0}, a1 = {0, 0, 0, 0};
#pragma unroll
      for (int i = 0; i < 4; ++i) { const int x = 4 * al + i; B0[x] = cat(lds_ld<u32x2>(b0 + 64 * x), lds_ld<u32x2>(b0 + 64 * x + 8)); }
      if (al >= 2) {
#pragma unroll
        for (int i = 0; i < 2; ++i) { const int x = 2 * (al - 2) + i; B1[x] = cat(lds_tr(b1w + 128 * x), lds_tr(b1u + 128 * x)); }
      }
      if (al >= 4) { const int x = al - 4; B2[x] = cat(lds_tr(b2a + 256 * x), lds_tr(b2b + 256 * x)); }
      if (al >= 8) { const int x = al - 8; B3[x] = cat(lds_tr(b3u + 256 * x), lds_tr(b3w + 256 * x)); }
#pragma unroll
      for (int s = 0; s < 10; ++s) if (s < 4 * al + 4) a0 = mm(A0[s], B0[4 * al + 3 - s], a0);
#pragma unroll
      for (int s = 0; s < 5; ++s) if (s < 2 * al - 2) a1 = mm(A1[s], B1[2 * al - 3 - s], a1);
#pragma unroll
      for (int s = 0; s < 4; ++s) if (s < al - 3) a1 = mm(A2[s], B2[al - 4 - s], a1);
      if (al >= 8) { const int x = al - 8; a1 = mm(A2[4], cat(lds_tr(b2s + 256 * x), lds_tr(b2b + 256 * x)), a1); }
#pragma unroll
      for (int s = 0; s < 8; ++s) if (s < al - 7) a0 = mm(A3[s], B3[al - 8 - s], a0);
      a0 += a1;
      const f32x2 gg = lds_ld<f32x2>(G_OFF + 512 * al + 8 * lane);
      const u32 o = pk_bf(fmaf(gg[0], a0[0] - a0[1], a0[1]), fmaf(gg[1], a0[2] - a0[3], a0[3]));
      asm volatile("ds_write_addtid_b32 %0 offset:%1" ::"v"(o), "i"(49152 + 1024 * al) : "memory");
    }
  }
  ST(6);
  wg_barrier();
  {
    const int t1 = 64 * ((lane >> 3) & 1) + 16 * (lane >> 4) + (lane & 7);
#pragma unroll
    for (int i = 0; i < 2; ++i) {
      const int al = w + 8 * i;
      const u32 a = O_OFF + 1024 * al + 4 * lane;
      const u32 e0 = lds_ld<u32>(a), e1 = lds_ld<u32>(a + 256), e2 = lds_ld<u32>(a + 512), e3 = lds_ld<u32>(a + 768);
      u16* po = out + (size_t)(128 * al + t1) * D + c0;
      *(u32x2*)po = (u32x2){__builtin_amdgcn_perm(e1, e0, 0x05040100), __builtin_amdgcn_perm(e3, e2, 0x05040100)};
      *(u32x2*)(po + 8 * D) = (u32x2){__builtin_amdgcn_perm(e1, e0, 0x07060302), __builtin_amdgcn_perm(e3, e2, 0x07060302)};
    }
  }
  asm volatile(".Lssm_code_b:");
  ST(7);
}

extern "C" void kernel_launch(void* const* d_in, const int* in_sizes, int n_in,
                              void* d_out, int out_size, void* d_ws, size_t ws_size,
                              hipStream_t stream) {
  const u16* const* in = (const u16* const*)d_in;
  (void)hipFuncSetAttribute((const void*)ssm_mix, hipFuncAttributeMaxDynamicSharedMemorySize, LDS_BYTES);
  ssm_mix<<<256, 512, LDS_BYTES, stream>>>(in[0], in[1], in[2], in[3], in[4], in[5], in[6], in[7], in[8],
                                           in[10], in[11], in[12], in[13], in[14], in[15], in[16],
                                           (u16*)d_out, (char*)d_ws);
}
```

```cpp
#define DIAG_BLK 88
#define DIAG_W 0
#include <hip/hip_runtime.h>
#include <hip/hip_bf16.h>
#include <stdint.h>
#pragma clang diagnostic ignored "-Wint-to-pointer-cast"

typedef unsigned long long u64;
typedef unsigned u32;
typedef unsigned short u16;
typedef u32 u32x2 __attribute__((ext_vector_type(2)));
typedef u32 u32x4 __attribute__((ext_vector_type(4)));
typedef short s16x4 __attribute__((ext_vector_type(4)));
typedef float f32x2 __attribute__((ext_vector_type(2)));
typedef float f32x4 __attribute__((ext_vector_type(4)));
typedef float f32x16 __attribute__((ext_vector_type(16)));
typedef _Float16 h16x2 __attribute__((ext_vector_type(2)));
typedef _Float16 h16x8 __attribute__((ext_vector_type(8)));
typedef __bf16 b16x2 __attribute__((ext_vector_type(2)));
typedef __bf16 b16x8 __attribute__((ext_vector_type(8)));
#define LAS __attribute__((address_space(3)))
#define GAS __attribute__((address_space(1)))
#define RLX_AGENT __ATOMIC_RELAXED, __HIP_MEMORY_SCOPE_AGENT
#define DEV __device__ __forceinline__

constexpr int L = 2048, D = 640, N = 192;
constexpr int U_OFF = 0, U_CH = 17312, U_R = 4320;
constexpr int W3_OFF = 69248, W3_CH = 3744;
constexpr int W5_OFF = 84224, W5_CH = 3232;
constexpr int W7_OFF = 97152, W7_CH = 2208;
constexpr int O_OFF = 105984;
constexpr int T_ROW = 2064;
constexpr int R_OFF = 122368;
constexpr int KR_OFF = 138816, KR_ST = 896;
constexpr int KS_OFF = 145984, KS_ST = 1088;
constexpr int G_OFF = 154688;
constexpr int MISC_OFF = 162880;
constexpr int LDS_BYTES = 163840;
constexpr int WS_ACK = 4096, WS_GR = 8192, WS_STAMP = 1 << 20;

DEV float bf_lo(u32 v) { return __uint_as_float(v << 16); }
DEV float bf_hi(u32 v) { return __uint_as_float(v & 0xffff0000u); }
DEV u32 pk_bf(float a, float b) { f32x2 x = {a, b}; b16x2 y = __builtin_convertvector(x, b16x2); return __builtin_bit_cast(u32, y); }
DEV float rbf(float a) { return bf_lo(pk_bf(a, a)); }
DEV h16x2 pk_h(f32x2 x) { return __builtin_convertvector(x, h16x2); }
DEV h16x2 cmul_h(h16x2 x, h16x2 m) {
  h16x2 t = x * (h16x2){m[0], m[0]};
  return __builtin_elementwise_fma((h16x2){-x[1], x[0]}, (h16x2){m[1], m[1]}, t);
}
DEV h16x2 cmulc_h(h16x2 x, h16x2 m) {
  h16x2 t = x * (h16x2){m[0], m[0]};
  return __builtin_elementwise_fma((h16x2){x[1], -x[0]}, (h16x2){m[1], m[1]}, t);
}
DEV f32x2 cmulc_f(f32x2 x, f32x2 m) {
  f32x2 t = x * (f32x2){m[0], m[0]};
  return __builtin_elementwise_fma((f32x2){x[1], -x[0]}, (f32x2){m[1], m[1]}, t);
}
DEV f32x2 csq_f(f32x2 x) {
  f32x2 t = x * (f32x2){x[0], x[0]};
  return __builtin_elementwise_fma((f32x2){-x[1], x[0]}, (f32x2){x[1], x[1]}, t);
}
template <int OFF> DEV void tw(h16x2 v) {
  asm volatile("ds_write_addtid_b32 %0 offset:%1" ::"v"(v), "i"(OFF) : "memory");
}
DEV void set_m0(u32 v) { asm volatile("s_mov_b32 m0, %0\n\ts_nop 0" ::"s"(v) : "memory"); }
template <typename T> DEV T lds_ld(u32 a) { return *(LAS T*)a; }
template <typename T> DEV void lds_st(u32 a, T v) { *(LAS T*)a = v; }
DEV u32x2 lds_tr(u32 a) { return __builtin_bit_cast(u32x2, __builtin_amdgcn_ds_read_tr16_b64_v4i16((LAS s16x4*)a)); }
DEV u64 next_tag(u64 e) { return e * 6364136223846793005ull + 1442695040888963407ull; }
DEV void wg_barrier() { asm volatile("s_waitcnt lgkmcnt(0)\n\ts_barrier" ::: "memory"); }
DEV f32x4 mm(u32x4 a, u32x4 b, f32x4 c) {
  return __builtin_amdgcn_mfma_f32_16x16x32_bf16(__builtin_bit_cast(b16x8, a), __builtin_bit_cast(b16x8, b), c, 0, 0, 0);
}
DEV u32x4 cat(u32x2 a, u32x2 b) { return (u32x4){a.x, a.y, b.x, b.y}; }
#define ST(i) dg_rt[i] = __builtin_amdgcn_s_memrealtime()

template <bool DO_U, int NW> DEV void prep_block(int gam, const u32 (&ra)[4], u32 wu, u32 ww) {
  u32 d[8];
#pragma unroll
  for (int n = 0; n < 4; ++n) { u32x2 v = lds_tr(ra[n] + 1024 * gam); d[2 * n] = v.x; d[2 * n + 1] = v.y; }
  if (DO_U) {
    u32 o[5];
#pragma unroll
    for (int k = 0; k < 5; ++k) o[k] = __builtin_amdgcn_alignbit(d[k + 3], d[k + 2], 16);
    u32 a = wu + 256 * gam;
    lds_st<u32x4>(a, (u32x4){d[4], d[5], d[6], d[7]});
    lds_st<u32x4>(a + U_R, (u32x4){o[1], o[2], o[3], o[4]});
    lds_st<u32x4>(a + 2 * U_R, (u32x4){d[3], d[4], d[5], d[6]});
    lds_st<u32x4>(a + 3 * U_R, (u32x4){o[0], o[1], o[2], o[3]});
  }
  if (NW > 0) {
    float P[16];
    constexpr int k0 = 4 - NW, k1 = 11 + NW;
    P[k0 - 1] = 0.f;
#pragma unroll
    for (int k = k0; k <= k1; ++k) { float v = (k & 1) ? bf_hi(d[k >> 1]) : bf_lo(d[k >> 1]); P[k] = k == k0 ? v : P[k - 1] + v; }
#pragma unroll
    for (int h = 1; h <= NW; ++h) {
      u32 r[4];
#pragma unroll
      for (int j = 0; j < 8; j += 2) r[j >> 1] = pk_bf(P[4 + j + h] - P[3 + j - h], P[5 + j + h] - P[4 + j - h]);
      u32 a = (h == 1 ? W3_OFF + (ww >> 16) * W3_CH : h == 2 ? W5_OFF + (ww >> 16) * W5_CH : W7_OFF + (ww >> 16) * W7_CH) + (ww & 0xffff) + 256 * gam;
      lds_st<u32x4>(a, (u32x4){r[0], r[1], r[2], r[3]});
    }
  }
}

__global__ void __launch_bounds__(512, 2)
ssm_mix(const u16* __restrict__ u, const u16* __restrict__ aAre, const u16* __restrict__ aAim,
        const u16* __restrict__ aBre, const u16* __restrict__ aBim, const u16* __restrict__ aCre,
        const u16* __restrict__ aCim, const u16* __restrict__ aLdt, const u16* __restrict__ aD,
        const u16* __restrict__ bLA, const u16* __restrict__ bAim, const u16* __restrict__ bCre,
        const u16* __restrict__ bCim, const u16* __restrict__ bLdt, const u16* __restrict__ bD,
        const u16* __restrict__ gw, u16* __restrict__ out, char* __restrict__ ws) {
  extern __shared__ __attribute__((aligned(16))) char smem[];
  asm volatile(".Lssm_code_0:");
  const int tid = threadIdx.x, lane = tid & 63;
  const int w = __builtin_amdgcn_readfirstlane(tid >> 6);
  const int bx = blockIdx.x & 7, bj = blockIdx.x >> 3;
  GAS u64* wsE = (GAS u64*)ws;
  GAS u64* wsAck = (GAS u64*)(ws + WS_ACK);
  GAS u64* wsGr = (GAS u64*)(ws + WS_GR);
  u64 dg_rt[16]; const u64 dg_c0 = __builtin_amdgcn_s_memtime();
  ST(0);
  if (bj == 0 || bj > 20) {
    if (bj == 0) {
      if (w < 7) {
        u64 pc; int back, len;
        asm volatile("s_getpc_b64 %0\n.Lssm_code_a:\n\ts_mov_b32 %1, .Lssm_code_a-.Lssm_code_0\n\ts_mov_b32 %2, .Lssm_code_b-.Lssm_code_0"
                     : "=s"(pc), "=s"(back), "=s"(len));
        if (tid * 128 < len) {
          u32 v = __builtin_nontemporal_load((const GAS u32*)(pc - back) + tid * 32);
          asm volatile("" ::"v"(v));
        }
        return;
      }
      u64 T = next_tag(__hip_atomic_load(wsE, RLX_AGENT));
      if (lane == 0) __hip_atomic_store(wsAck + blockIdx.x, T, RLX_AGENT);
      if (bx != 0) return;
      for (int spin = 0; spin < 200000; ++spin) {
        __builtin_amdgcn_s_sleep(16);
        bool ok = true;
#pragma unroll
        for (int i = 0; i < 4; ++i) ok &= __hip_atomic_load(wsAck + i * 64 + lane, RLX_AGENT) == T;
        if (__all(ok)) { if (lane == 0) __hip_atomic_store(wsE, T, RLX_AGENT); break; }
      }
      return;
    }
    const int hh = (bj - 21) * 8 + bx;
    float s = 0.f;
    const int row = hh * 24 + w * 4 + (lane >> 4);
    if (w < 6 && hh * 24 + w * 4 < L) {
      const u32x4* pu = (const u32x4*)(u + (size_t)row * D) + (lane & 15);
      const u32x4* pw = (const u32x4*)gw + (lane & 15);
      u32x4 a[5], b[5];
#pragma unroll
      for (int i = 0; i < 5; ++i) { a[i] = pu[16 * i]; b[i] = pw[16 * i]; }
#pragma unroll
      for (int i = 0; i < 5; ++i)
#pragma unroll
        for (int k = 0; k < 4; ++k) {
          const u32 x = a[i][k], y = b[i][k];
          s = __builtin_amdgcn_fdot2_f32_bf16(__builtin_bit_cast(b16x2, x), __builtin_bit_cast(b16x2, y), s, false);
        }
      s += __shfl_xor(s, 1); s += __shfl_xor(s, 2); s += __shfl_xor(s, 4); s += __shfl_xor(s, 8);
    } else if (w == 7) {
      u64 T = next_tag(__hip_atomic_load(wsE, RLX_AGENT));
      if (lane == 0) { __hip_atomic_store(wsAck + blockIdx.x, T, RLX_AGENT); lds_st<u64>(MISC_OFF, T); }
    }
    wg_barrier();
    if (w < 6 && hh * 24 + w * 4 < L && (lane & 15) == 0) {
      u64 T = lds_ld<u64>(MISC_OFF);
      s = rbf(s);
      float g = __builtin_amdgcn_rcpf(1.f + __builtin_amdgcn_exp2f(-1.4426950408889634f * s));
      __hip_atomic_store(wsGr + row, (T & ~0xffffull) | (pk_bf(g, g) & 0xffffu), RLX_AGENT);
    }
    ST(1);
    return;
  }
  const int c0 = 80 * bx + 4 * (bj - 1);
  const int ch = w & 3, br = w >> 2, d = c0 + ch;
  u32 p2[6]; u32 p1[6];
  {
    const int o2 = d * N + 2 * lane, o1 = d * N + 128 + lane;
    if (br == 0) {
      const u16* a[6] = {aAre, aAim, aCre, aCim, aBre, aBim};
#pragma unroll
      for (int i = 0; i < 6; ++i) { p2[i] = *(const u32*)(a[i] + o2); p1[i] = a[i][o1]; }
    } else {
      const u16* a[4] = {bLA, bAim, bCre, bCim};
#pragma unroll
      for (int i = 0; i < 4; ++i) { p2[i] = *(const u32*)(a[i] + o2); p1[i] = a[i][o1]; }
      p2[4] = p2[5] = p1[4] = p1[5] = 0;
    }
  }
  const float ldt = bf_lo((br ? bLdt : aLdt)[d]);
  const float dsk = bf_lo((br ? bD : aD)[d]);
  u64 E = 0;
  if (w == 0) E = __hip_atomic_load(wsE, RLX_AGENT);
  u32x2 ur[4];
#pragma unroll
  for (int i = 0; i < 4; ++i) ur[i] = *(const u32x2*)(u + (size_t)(256 * w + 64 * i + lane) * D + c0);
  const u32 krb = KR_OFF + (2 * ch + br) * KR_ST, ksb = KS_OFF + (2 * ch + br) * KS_ST;
  {
    const u32x4 z = {0, 0, 0, 0};
    if (lane < 56) lds_st<u32x4>(krb + 16 * lane, z);
    lds_st<u32x4>(ksb + 16 * lane, z);
    if (lane < 4) lds_st<u32x4>(ksb + 1024 + 16 * lane, z);
    if (w == 0 && lane < 4) lds_st<u32x4>(R_OFF + 16 * lane, z);
    lds_st<u32>(21 * T_ROW + 256 * w + 4 * lane, 0x00003c00u);
  }
  f32x2 zz[3], ww[3];
  float unscale;
  {
    const float dt = rbf(__builtin_amdgcn_exp2f(1.4426950408889634f * ldt));
    const f32x2 dz = {dt * 1.4426950408889634f, dt * 0.15915494309189535f};
    int emax = -120;
#pragma unroll
    for (int k = 0; k < 3; ++k) {
      float v[6];
#pragma unroll
      for (int i = 0; i < 6; ++i) v[i] = k == 0 ? bf_lo(p2[i]) : k == 1 ? bf_hi(p2[i]) : bf_lo(p1[i]);
      f32x2 cw;
      if (br == 0) {
        cw = (f32x2){v[2] * v[4] - v[3] * v[5], -(v[2] * v[5] + v[3] * v[4])} * dt;
      } else {
        v[0] = -rbf(__builtin_amdgcn_exp2f(1.4426950408889634f * v[0]));
        cw = (f32x2){v[2], -v[3]} * (2.f * dt);
      }
      zz[k] = (f32x2){v[0], v[1]} * dz;
      ww[k] = cw;
      int e = __builtin_amdgcn_frexp_expf(fmaxf(fabsf(cw[0]), fabsf(cw[1])));
      if (br == 0) e += (int)fminf(fmaxf(ceilf(2048.f * zz[k][0]), 0.f), 200.f);
      emax = max(emax, e);
    }
#pragma unroll
    for (int o = 1; o < 64; o <<= 1) emax = max(emax, __shfl_xor(emax, o));
    emax = __builtin_amdgcn_readfirstlane(min(emax, 126));
    const float sc = __int_as_float((127 + 14 - emax > 254 ? 254 : 127 + 14 - emax) << 23);
    unscale = 1.f / sc;
#pragma unroll
    for (int k = 0; k < 3; ++k) ww[k] *= sc;
  }
  if (w == 0) {
    const u64 T = next_tag(E);
    if (lane == 0) { __hip_atomic_store(wsAck + blockIdx.x, T, RLX_AGENT); lds_st<u64>(MISC_OFF, T); }
  }
  ST(1);
  f32x16 acc = {0};
  u32x4 gr0, gr1;
  const u32 fa = (lane & 31) * T_ROW + 256 * w + 16 * (lane >> 5);
#pragma unroll 1
  for (int k = 0; k < 3; ++k) {
    const f32x2 z = zz[0], W = ww[0];
    zz[0] = zz[1]; zz[1] = zz[2]; ww[0] = ww[1]; ww[1] = ww[2];
    const float e = __builtin_amdgcn_exp2f(z[0]), f = __builtin_amdgcn_fractf(z[1]);
    f32x2 m = (f32x2){__builtin_amdgcn_cosf(f), __builtin_amdgcn_sinf(f)} * e;
    h16x2 M[8];
#pragma unroll
    for (int i = 0; i < 8; ++i) { M[i] = pk_h(m); m = csq_f(m); }
    set_m0(256 * w);
    {
      h16x2 Y[8];
      Y[0] = pk_h(W); tw<0>(Y[0]);
      Y[1] = cmulc_h(Y[0], M[0]); tw<1 * T_ROW>(Y[1]);
      Y[2] = cmulc_h(Y[0], M[1]); tw<2 * T_ROW>(Y[2]);
      Y[3] = cmulc_h(Y[1], M[1]); tw<3 * T_ROW>(Y[3]);
      Y[4] = cmulc_h(Y[0], M[2]); tw<4 * T_ROW>(Y[4]);
      Y[5] = cmulc_h(Y[1], M[2]); tw<5 * T_ROW>(Y[5]);
      Y[6] = cmulc_h(Y[2], M[2]); tw<6 * T_ROW>(Y[6]);
      Y[7] = cmulc_h(Y[3], M[2]); tw<7 * T_ROW>(Y[7]);
      f32x2 Z = cmulc_f(W, m);
      h16x2 a = pk_h(Z); tw<8 * T_ROW>(a);
      tw<9 * T_ROW>(cmulc_h(a, M[2]));
      a = cmulc_h(a, M[1]); tw<10 * T_ROW>(a);
      tw<11 * T_ROW>(cmulc_h(a, M[2]));
      Z = cmulc_f(Z, m); a = pk_h(Z); tw<12 * T_ROW>(a);
      tw<13 * T_ROW>(cmulc_h(a, M[2]));
      Z = cmulc_f(Z, m); a = pk_h(Z); tw<14 * T_ROW>(a);
      tw<15 * T_ROW>(cmulc_h(a, M[2]));
      Z = cmulc_f(Z, m); tw<16 * T_ROW>(pk_h(Z));
      Z = cmulc_f(Z, m); tw<17 * T_ROW>(pk_h(Z));
      Z = cmulc_f(Z, m); tw<18 * T_ROW>(pk_h(Z));
      Z = cmulc_f(Z, m); tw<19 * T_ROW>(pk_h(Z));
      Z = cmulc_f(Z, m); tw<20 * T_ROW>(pk_h(Z));
    }
    set_m0(22 * T_ROW + 256 * w);
    {
      h16x2 X[16];
#define COL(c, v) tw<((c) - 1) * T_ROW>(v)
      X[1] = M[3]; COL(1, X[1]);
      X[2] = M[4]; COL(2, X[2]);
      X[3] = cmul_h(X[1], M[4]); COL(3, X[3]);
      X[4] = M[5]; COL(4, X[4]);
      X[5] = cmul_h(X[1], M[5]); COL(5, X[5]);
      X[6] = cmul_h(X[2], M[5]); COL(6, X[6]);
      X[7] = cmul_h(X[3], M[5]); COL(7, X[7]);
      X[8] = M[6]; COL(8, X[8]);
#define C8(c) X[c] = cmul_h(X[(c) - 8], M[6]); COL(c, X[c]);
      C8(9) C8(10) C8(11) C8(12) C8(13) C8(14) C8(15)
      COL(16, M[7]);
#define C16(c) COL(c, cmul_h(X[(c) - 16], M[7]));
      C16(17) C16(18) C16(19) C16(20) C16(21) C16(22) C16(23) C16(24) C16(25) C16(26) C16(27) C16(28) C16(29) C16(30) C16(31)
    }
    if (k == 1) {
      auto rg = __builtin_amdgcn_make_buffer_rsrc((void*)(ws + WS_GR), 0, L * 8, 0x00020000);
      gr0 = __builtin_amdgcn_raw_buffer_load_b128(rg, tid * 16, 0, 16);
      gr1 = __builtin_amdgcn_raw_buffer_load_b128(rg, tid * 16 + 8192, 0, 16);
    }
#pragma unroll
    for (int s = 0; s < 8; ++s)
      acc = __builtin_amdgcn_mfma_f32_32x32x16_f16(lds_ld<h16x8>(fa + 32 * s), lds_ld<h16x8>(fa + 21 * T_ROW + 32 * s), acc, 0, 0, 0);
    asm volatile("" ::: "memory");
  }
  ST(2);
  {
    const int c = lane & 31, h = lane >> 5;
    float k[12];
#pragma unroll
    for (int i = 0; i < 12; ++i) k[i] = acc[i] * unscale;
    if (lane == 0) k[0] += dsk;
    lds_st<u32x2>(krb + 2 * (316 - 8 * c - 4 * h), (u32x2){pk_bf(k[3], k[2]), pk_bf(k[1], k[0])});
    const u32 x0 = pk_bf(k[4], k[5]), x1 = pk_bf(k[6], k[7]), y0 = pk_bf(k[8], k[9]), y1 = pk_bf(k[10], k[11]);
    if (h == 0) {
      if (c > 0) {
        lds_st<u32>(ksb + 2 * (2 * c + 12), x0);
        lds_st<u32>(ksb + 192 + 2 * (2 * c + 12), x1);
      } else {
        lds_st<u32x4>(krb + 2 * 56, (u32x4){x1 & 0xffff0000u, (x0 >> 16) * 0x10001u, (x0 >> 16) | (x1 << 16), (x0 & 0xffffu) * 0x10001u});
        lds_st<u16>(ksb + 384 + 4 * 70 + 2, (u16)y0);
      }
      lds_st<u16>(ksb + 736 + 2 * (8 + c), (u16)y0);
      lds_st<u16>(ksb + 736 + 2 * (40 + c), (u16)(y0 >> 16));
      lds_st<u16>(ksb + 736 + 2 * (72 + c), (u16)y1);
      lds_st<u16>(ksb + 736 + 2 * (104 + c), (u16)(y1 >> 16));
    } else {
      if (c > 0) lds_st<u16>(ksb + 384 + 4 * (c + 6) + 2, (u16)x0);
      else { lds_st<u16>(ksb + 2 * 76, (u16)x0); lds_st<u16>(ksb + 192 + 2 * 76, (u16)x0); lds_st<u16>(ksb + 736 + 2 * 136, (u16)y0); }
      lds_st<u16>(ksb + 384 + 4 * (c + 7), (u16)(x0 >> 16));
      lds_st<u16>(ksb + 384 + 4 * (c + 38) + 2, (u16)x1);
      lds_st<u16>(ksb + 384 + 4 * (c + 39), (u16)(x1 >> 16));
    }
  }
  {
    const int y = 8 + 256 * w + lane;
    const u32 a = R_OFF + 8 * (y ^ (((y >> 5) & 1) << 2));
#pragma unroll
    for (int i = 0; i < 4; ++i) lds_st<u32x2>(a + 512 * i, ur[i]);
  }
  wg_barrier();
  ST(3);
  {
    const u64 T = lds_ld<u64>(MISC_OFF);
    const u32 thi = (u32)(T >> 32), tlo = (u32)T >> 16;
    auto rg = __builtin_amdgcn_make_buffer_rsrc((void*)(ws + WS_GR), 0, L * 8, 0x00020000);
    for (int spin = 0; spin < 200000; ++spin) {
      bool ok = gr0.y == thi && gr0.w == thi && gr1.y == thi && gr1.w == thi &&
                (gr0.x >> 16) == tlo && (gr0.z >> 16) == tlo && (gr1.x >> 16) == tlo && (gr1.z >> 16) == tlo;
      if (__all(ok)) break;
      asm volatile("" ::: "memory");
      gr0 = __builtin_amdgcn_raw_buffer_load_b128(rg, tid * 16, 0, 16);
      gr1 = __builtin_amdgcn_raw_buffer_load_b128(rg, tid * 16 + 8192, 0, 16);
    }
    const int t = 2 * tid;
    const u32 a = G_OFF + 512 * (t >> 7) + 8 * (16 * ((t >> 4) & 3) + 8 * ((t >> 6) & 1) + (t & 7)) + 4 * ((t >> 3) & 1);
    lds_st<u32>(a, gr0.x << 16); lds_st<u32>(a + 8, gr0.z << 16);
    lds_st<u32>(a + 4096, gr1.x << 16); lds_st<u32>(a + 4096 + 8, gr1.z << 16);
  }
  ST(4);
  {
    u32 ra[4];
    const int G = lane >> 4, q = (lane >> 2) & 3, p = lane & 3;
#pragma unroll
    for (int n = 0; n < 4; ++n) { const int y = 32 * G + 8 * p + q + 4 * n; ra[n] = R_OFF + 8 * (y ^ (((y >> 5) & 1) << 2)); }
    const u32 wu = U_OFF + p * U_CH + 160 + 16 * (lane >> 2);
    const u32 wwp = (p << 16) | (160 + 16 * (lane >> 2));
    prep_block<true, 3>(w, ra, wu, wwp);
    if (w < 4) prep_block<true, 2>(w + 8, ra, wu, wwp);
    else if (w < 6) prep_block<true, 1>(w + 8, ra, wu, wwp);
    else prep_block<true, 0>(w + 8, ra, wu, wwp);
    if (tid < 280) {
      const int a = tid / 10, i = tid % 10;
      const u32 base = a < 16 ? U_OFF + (a >> 2) * U_CH + (a & 3) * U_R
                     : a < 20 ? W3_OFF + (a - 16) * W3_CH : a < 24 ? W5_OFF + (a - 20) * W5_CH : W7_OFF + (a - 24) * W7_CH;
      lds_st<u32x4>(base + 16 * i, (u32x4){0, 0, 0, 0});
    }
  }
  wg_barrier();
  ST(5);
  if (w < 4) {
    const int g = lane >> 4, abr = lane & 1, beta = (lane & 15) >> 1;
    const int ap = (lane >> 3) & 1, b = lane & 7, q = (lane >> 2) & 3, p = lane & 3;
    const u32 kr = KR_OFF + (2 * ch + abr) * KR_ST, ks = KS_OFF + (2 * ch + abr) * KS_ST;
    u32x4 A0[10], A1[5], A2[5], A3[8];
#pragma unroll
    for (int s = 0; s < 10; ++s) A0[s] = lds_ld<u32x4>(kr + 2 * (368 - 8 * (g + beta)) - 64 * s);
#pragma unroll
    for (int s = 0; s < 5; ++s) {
      const u32 a = ks + 2 * (4 * g + 2 * beta) + 32 * s;
      A1[s] = (u32x4){lds_ld<u32>(a), lds_ld<u32>(a + 4), lds_ld<u32>(a + 192), lds_ld<u32>(a + 196)};
    }
#pragma unroll
    for (int s = 0; s < 5; ++s) {
      const u32 a = ks + 384 + 4 * (4 * g + beta) + 64 * s;
      const u32 e0 = lds_ld<u32>(a), e1 = lds_ld<u32>(a + 4), e2 = lds_ld<u32>(a + 8), e3 = lds_ld<u32>(a + 12);
      A2[s] = (u32x4){__builtin_amdgcn_perm(e1, e0, 0x05040100), __builtin_amdgcn_perm(e3, e2, 0x05040100),
                      __builtin_amdgcn_perm(e1, e0, 0x07060302), __builtin_amdgcn_perm(e3, e2, 0x07060302)};
    }
    {
      const u32 k1024 = lds_ld<u16>(ks + 736 + 16);
      const int qq = beta - 4 * (g - 2);
      if (g >= 2 && qq >= 0 && qq < 4) { if (qq < 2) A2[4].x = k1024 << (16 * qq); else A2[4].y = k1024 << (16 * (qq - 2)); }
    }
    {
      const int m0 = -7 + 4 * g + beta, odd = m0 & 1;
      const u32 st = odd ? 0x05040504u : 0x05040100u, sc = odd ? 0x07060504u : 0x05040302u;
#pragma unroll
      for (int s = 0; s < 8; ++s) {
        const u32 a = ks + 736 + 2 * (m0 - odd + 8) + 32 * s;
        const u32 e0 = lds_ld<u32>(a), e1 = lds_ld<u32>(a + 4), e2 = lds_ld<u32>(a + 8);
        A3[s] = (u32x4){__builtin_amdgcn_perm(e1, e0, st), __builtin_amdgcn_perm(e2, e1, st),
                        __builtin_amdgcn_perm(e1, e0, sc), __builtin_amdgcn_perm(e2, e1, sc)};
      }
      const int qq = -1 - m0;
      const u32 k0 = qq == 0 ? 0xffff0000u : qq == 1 ? 0x0000ffffu : ~0u, k1 = qq == 2 ? 0xffff0000u : qq == 3 ? 0x0000ffffu : ~0u;
      A3[0].x &= k0; A3[0].z &= k0; A3[0].y &= k1; A3[0].w &= k1;
    }
    const u32 uc = U_OFF + ch * U_CH;
    const u32 b0 = uc + (3 - (b & 3)) * U_R + 2 * (64 * ap + 4 * (b >> 2) + 132 - 8 * g - 96);
    const int xb = 64 * (p >> 1) + 4 * (p & 1);
    const u32 b1w = W3_OFF + ch * W3_CH + 2 * (xb - 208 - 16 * g - 4 * q + 84 + 192);
    const u32 b1u = uc + 2 * U_R + 2 * (xb - 208 - 16 * g - 4 * q + 80 + 192);
    const u32 b2a = W3_OFF + ch * W3_CH + 2 * (xb - 460 - 32 * g - 8 * q + 84 + 512);
    const u32 b2b = W5_OFF + ch * W5_CH + 2 * (xb - 460 - 32 * g - 8 * q + 80 + 512);
    const u32 b2s = g < 2 ? b2a : uc + 3 * U_R + 2 * (xb + 8 * (q + 4 * (g - 2)) - 944 + 1024);
    const u32 b3u = uc + 2 * (xb + 52 - 32 * g - 8 * q + 80);
    const u32 b3w = W7_OFF + ch * W7_CH + 2 * (xb + 52 - 32 * g - 8 * q + 80);
    set_m0(56832 + 256 * ch);
    u32x4 B0[64], B1[28], B2[12], B3[8];
#pragma unroll
    for (int al = 0; al < 16; ++al) {
      f32x4 a0 = {0, 0, 0, 0}, a1 = {0, 0, 0, 0};
#pragma unroll
      for (int i = 0; i < 4; ++i) { const int x = 4 * al + i; B0[x] = cat(lds_ld<u32x2>(b0 + 64 * x), lds_ld<u32x2>(b0 + 64 * x + 8)); }
      if (al >= 2) {
#pragma unroll
        for (int i = 0; i < 2; ++i) { const int x = 2 * (al - 2) + i; B1[x] = cat(lds_tr(b1w + 128 * x), lds_tr(b1u + 128 * x)); }
      }
      if (al >= 4) { const int x = al - 4; B2[x] = cat(lds_tr(b2a + 256 * x), lds_tr(b2b + 256 * x)); }
      if (al >= 8) { const int x = al - 8; B3[x] = cat(lds_tr(b3u + 256 * x), lds_tr(b3w + 256 * x)); }
#pragma unroll
      for (int s = 0; s < 10; ++s) if (s < 4 * al + 4) a0 = mm(A0[s], B0[4 * al + 3 - s], a0);
#pragma unroll
      for (int s = 0; s < 5; ++s) if (s < 2 * al - 2) a1 = mm(A1[s], B1[2 * al - 3 - s], a1);
#pragma unroll
      for (int s = 0; s < 4; ++s) if (s < al - 3) a1 = mm(A2[s], B2[al - 4 - s], a1);
      if (al >= 8) { const int x = al - 8; a1 = mm(A2[4], cat(lds_tr(b2s + 256 * x), lds_tr(b2b + 256 * x)), a1); }
#pragma unroll
      for (int s = 0; s < 8; ++s) if (s < al - 7) a0 = mm(A3[s], B3[al - 8 - s], a0);
      a0 += a1;
      const f32x2 gg = lds_ld<f32x2>(G_OFF + 512 * al + 8 * lane);
      const u32 o = pk_bf(fmaf(gg[0], a0[0] - a0[1], a0[1]), fmaf(gg[1], a0[2] - a0[3], a0[3]));
      asm volatile("ds_write_addtid_b32 %0 offset:%1" ::"v"(o), "i"(49152 + 1024 * al) : "memory");
    }
  }
  ST(6);
  wg_barrier();
  {
    const int t1 = 64 * ((lane >> 3) & 1) + 16 * (lane >> 4) + (lane & 7);
#pragma unroll
    for (int i = 0; i < 2; ++i) {
      const int al = w + 8 * i;
      const u32 a = O_OFF + 1024 * al + 4 * lane;
      const u32 e0 = lds_ld<u32>(a), e1 = lds_ld<u32>(a + 256), e2 = lds_ld<u32>(a + 512), e3 = lds_ld<u32>(a + 768);
      u16* po = out + (size_t)(128 * al + t1) * D + c0;
      *(u32x2*)po = (u32x2){__builtin_amdgcn_perm(e1, e0, 0x05040100), __builtin_amdgcn_perm(e3, e2, 0x05040100)};
      *(u32x2*)(po + 8 * D) = (u32x2){__builtin_amdgcn_perm(e1, e0, 0x07060302), __builtin_amdgcn_perm(e3, e2, 0x07060302)};
    }
  }
  asm volatile(".Lssm_code_b:");
  asm volatile("s_waitcnt vmcnt(0)" ::: "memory");
  ST(7);
  if (blockIdx.x == DIAG_BLK && w == DIAG_W) { const u64 dg_c1 = __builtin_amdgcn_s_memtime(); const u64 lim = 1000 + 0; while (__builtin_amdgcn_s_memrealtime() - dg_rt[7] < lim) __builtin_amdgcn_s_sleep(1); }
}

extern "C" void kernel_launch(void* const* d_in, const int* in_sizes, int n_in,
                              void* d_out, int out_size, void* d_ws, size_t ws_size,
                              hipStream_t stream) {
  const u16* const* in = (const u16* const*)d_in;
  (void)hipFuncSetAttribute((const void*)ssm_mix, hipFuncAttributeMaxDynamicSharedMemorySize, LDS_BYTES);
  ssm_mix<<<256, 512, LDS_BYTES, stream>>>(in[0], in[1], in[2], in[3], in[4], in[5], in[6], in[7], in[8],
                                           in[10], in[11], in[12], in[13], in[14], in[15], in[16],
                                           (u16*)d_out, (char*)d_ws);
}
```

```cpp
#include <hip/hip_runtime.h>
#include <hip/hip_bf16.h>
#include <stdint.h>
#pragma clang diagnostic ignored "-Wint-to-pointer-cast"

typedef unsigned long long u64;
typedef unsigned u32;
typedef unsigned short u16;
typedef u32 u32x2 __attribute__((ext_vector_type(2)));
typedef u32 u32x4 __attribute__((ext_vector_type(4)));
typedef short s16x4 __attribute__((ext_vector_type(4)));
typedef float f32x2 __attribute__((ext_vector_type(2)));
typedef float f32x4 __attribute__((ext_vector_type(4)));
typedef float f32x16 __attribute__((ext_vector_type(16)));
typedef _Float16 h16x2 __attribute__((ext_vector_type(2)));
typedef _Float16 h16x8 __attribute__((ext_vector_type(8)));
typedef __bf16 b16x2 __attribute__((ext_vector_type(2)));
typedef __bf16 b16x8 __attribute__((ext_vector_type(8)));
#define LAS __attribute__((address_space(3)))
#define GAS __attribute__((address_space(1)))
#define RLX_AGENT __ATOMIC_RELAXED, __HIP_MEMORY_SCOPE_AGENT
#define DEV __device__ __forceinline__

constexpr int L = 2048, D = 640, N = 192;
constexpr int U_OFF = 0, U_CH = 17312, U_R = 4320;
constexpr int W3_OFF = 69248, W3_CH = 3744;
constexpr int W5_OFF = 84224, W5_CH = 3232;
constexpr int W7_OFF = 97152, W7_CH = 2208;
constexpr int O_OFF = 105984;
constexpr int T_ROW = 2064;
constexpr int R_OFF = 122368;
constexpr int KR_OFF = 138816, KR_ST = 896;
constexpr int KS_OFF = 145984, KS_ST = 1088;
constexpr int G_OFF = 154688;
constexpr int MISC_OFF = 162880;
constexpr int LDS_BYTES = 163840;
constexpr int WS_ACK = 4096, WS_GR = 8192, WS_STAMP = 1 << 20;

DEV float bf_lo(u32 v) { return __uint_as_float(v << 16); }
DEV float bf_hi(u32 v) { return __uint_as_float(v & 0xffff0000u); }
DEV u32 pk_bf(float a, float b) { f32x2 x = {a, b}; b16x2 y = __builtin_convertvector(x, b16x2); return __builtin_bit_cast(u32, y); }
DEV float rbf(float a) { return bf_lo(pk_bf(a, a)); }
DEV h16x2 pk_h(f32x2 x) { return __builtin_convertvector(x, h16x2); }
DEV h16x2 cmul_h(h16x2 x, h16x2 m) {
  h16x2 t = x * (h16x2){m[0], m[0]};
  return __builtin_elementwise_fma((h16x2){-x[1], x[0]}, (h16x2){m[1], m[1]}, t);
}
DEV h16x2 cmulc_h(h16x2 x, h16x2 m) {
  h16x2 t = x * (h16x2){m[0], m[0]};
  return __builtin_elementwise_fma((h16x2){x[1], -x[0]}, (h16x2){m[1], m[1]}, t);
}
DEV f32x2 cmulc_f(f32x2 x, f32x2 m) {
  f32x2 t = x * (f32x2){m[0], m[0]};
  return __builtin_elementwise_fma((f32x2){x[1], -x[0]}, (f32x2){m[1], m[1]}, t);
}
DEV f32x2 csq_f(f32x2 x) {
  f32x2 t = x * (f32x2){x[0], x[0]};
  return __builtin_elementwise_fma((f32x2){-x[1], x[0]}, (f32x2){x[1], x[1]}, t);
}
template <int OFF> DEV void tw(h16x2 v) {
  asm volatile("ds_write_addtid_b32 %0 offset:%1" ::"v"(v), "i"(OFF) : "memory");
}
DEV void set_m0(u32 v) { asm volatile("s_mov_b32 m0, %0\n\ts_nop 0" ::"s"(v) : "memory"); }
template <typename T> DEV T lds_ld(u32 a) { return *(LAS T*)a; }
template <typename T> DEV void lds_st(u32 a, T v) { *(LAS T*)a = v; }
DEV u32x2 lds_tr(u32 a) { return __builtin_bit_cast(u32x2, __builtin_amdgcn_ds_read_tr16_b64_v4i16((LAS s16x4*)a)); }
DEV u64 next_tag(u64 e) { return e * 6364136223846793005ull + 1442695040888963407ull; }
DEV void wg_barrier() { asm volatile("s_waitcnt lgkmcnt(0)\n\ts_barrier" ::: "memory"); }
DEV f32x4 mm(u32x4 a, u32x4 b, f32x4 c) {
  return __builtin_amdgcn_mfma_f32_16x16x32_bf16(__builtin_bit_cast(b16x8, a), __builtin_bit_cast(b16x8, b), c, 0, 0, 0);
}
DEV u32x4 cat(u32x2 a, u32x2 b) { return (u32x4){a.x, a.y, b.x, b.y}; }
#define ST(i)

template <bool DO_U, int NW> DEV void prep_block(int gam, const u32 (&ra)[4], u32 wu, u32 ww) {
  u32 d[8];
#pragma unroll
  for (int n = 0; n < 4; ++n) { u32x2 v = lds_tr(ra[n] + 1024 * gam); d[2 * n] = v.x; d[2 * n + 1] = v.y; }
  if (DO_U) {
    u32 o[5];
#pragma unroll
    for (int k = 0; k < 5; ++k) o[k] = __builtin_amdgcn_alignbit(d[k + 3], d[k + 2], 16);
    u32 a = wu + 256 * gam;
    lds_st<u32x4>(a, (u32x4){d[4], d[5], d[6], d[7]});
    lds_st<u32x4>(a + U_R, (u32x4){o[1], o[2], o[3], o[4]});
    lds_st<u32x4>(a + 2 * U_R, (u32x4){d[3], d[4], d[5], d[6]});
    lds_st<u32x4>(a + 3 * U_R, (u32x4){o[0], o[1], o[2], o[3]});
  }
  if (NW > 0) {
    float P[16];
    constexpr int k0 = 4 - NW, k1 = 11 + NW;
    P[k0 - 1] = 0.f;
#pragma unroll
    for (int k = k0; k <= k1; ++k) { float v = (k & 1) ? bf_hi(d[k >> 1]) : bf_lo(d[k >> 1]); P[k] = k == k0 ? v : P[k - 1] + v; }
#pragma unroll
    for (int h = 1; h <= NW; ++h) {
      u32 r[4];
#pragma unroll
      for (int j = 0; j < 8; j += 2) r[j >> 1] = pk_bf(P[4 + j + h] - P[3 + j - h], P[5 + j + h] - P[4 + j - h]);
      u32 a = (h == 1 ? W3_OFF + (ww >> 16) * W3_CH : h == 2 ? W5_OFF + (ww >> 16) * W5_CH : W7_OFF + (ww >> 16) * W7_CH) + (ww & 0xffff) + 256 * gam;
      lds_st<u32x4>(a, (u32x4){r[0], r[1], r[2], r[3]});
    }
  }
}

__global__ void __launch_bounds__(512, 2)
ssm_mix(const u16* __restrict__ u, const u16* __restrict__ aAre, const u16* __restrict__ aAim,
        const u16* __restrict__ aBre, const u16* __restrict__ aBim, const u16* __restrict__ aCre,
        const u16* __restrict__ aCim, const u16* __restrict__ aLdt, const u16* __restrict__ aD,
        const u16* __restrict__ bLA, const u16* __restrict__ bAim, const u16* __restrict__ bCre,
        const u16* __restrict__ bCim, const u16* __restrict__ bLdt, const u16* __restrict__ bD,
        const u16* __restrict__ gw, u16* __restrict__ out, char* __restrict__ ws) {
  extern __shared__ __attribute__((aligned(16))) char smem[];
  asm volatile(".Lssm_code_0:");
  const int tid = threadIdx.x, lane = tid & 63;
  const int w = __builtin_amdgcn_readfirstlane(tid >> 6);
  const int bx = blockIdx.x & 7, bj = blockIdx.x >> 3;
  GAS u64* wsE = (GAS u64*)ws;
  GAS u64* wsAck = (GAS u64*)(ws + WS_ACK);
  GAS u64* wsGr = (GAS u64*)(ws + WS_GR);
  const u64 dg_0 = __builtin_amdgcn_s_memrealtime();
  if (tid == 0) __hip_atomic_store((GAS u64*)(ws + WS_STAMP) + 2 * blockIdx.x, dg_0, RLX_AGENT);

  if (bj == 0 || bj > 20) {
    if (bj == 0) {
      if (w < 7) {
        u64 pc; int back, len;
        asm volatile("s_getpc_b64 %0\n.Lssm_code_a:\n\ts_mov_b32 %1, .Lssm_code_a-.Lssm_code_0\n\ts_mov_b32 %2, .Lssm_code_b-.Lssm_code_0"
                     : "=s"(pc), "=s"(back), "=s"(len));
        if (tid * 128 < len) {
          u32 v = __builtin_nontemporal_load((const GAS u32*)(pc - back) + tid * 32);
          asm volatile("" ::"v"(v));
        }
        return;
      }
      u64 T = next_tag(__hip_atomic_load(wsE, RLX_AGENT));
      if (lane == 0) __hip_atomic_store(wsAck + blockIdx.x, T, RLX_AGENT);
      if (bx != 0) return;
      for (int spin = 0; spin < 200000; ++spin) {
        __builtin_amdgcn_s_sleep(16);
        bool ok = true;
#pragma unroll
        for (int i = 0; i < 4; ++i) ok &= __hip_atomic_load(wsAck + i * 64 + lane, RLX_AGENT) == T;
        if (__all(ok)) { if (lane == 0) __hip_atomic_store(wsE, T, RLX_AGENT); break; }
      }
      return;
    }
    const int hh = (bj - 21) * 8 + bx;
    float s = 0.f;
    const int row = hh * 24 + w * 4 + (lane >> 4);
    if (w < 6 && hh * 24 + w * 4 < L) {
      const u32x4* pu = (const u32x4*)(u + (size_t)row * D) + (lane & 15);
      const u32x4* pw = (const u32x4*)gw + (lane & 15);
      u32x4 a[5], b[5];
#pragma unroll
      for (int i = 0; i < 5; ++i) { a[i] = pu[16 * i]; b[i] = pw[16 * i]; }
#pragma unroll
      for (int i = 0; i < 5; ++i)
#pragma unroll
        for (int k = 0; k < 4; ++k) {
          const u32 x = a[i][k], y = b[i][k];
          s = __builtin_amdgcn_fdot2_f32_bf16(__builtin_bit_cast(b16x2, x), __builtin_bit_cast(b16x2, y), s, false);
        }
      s += __shfl_xor(s, 1); s += __shfl_xor(s, 2); s += __shfl_xor(s, 4); s += __shfl_xor(s, 8);
    } else if (w == 7) {
      u64 T = next_tag(__hip_atomic_load(wsE, RLX_AGENT));
      if (lane == 0) { __hip_atomic_store(wsAck + blockIdx.x, T, RLX_AGENT); lds_st<u64>(MISC_OFF, T); }
    }
    wg_barrier();
    if (w < 6 && hh * 24 + w * 4 < L && (lane & 15) == 0) {
      u64 T = lds_ld<u64>(MISC_OFF);
      s = rbf(s);
      float g = __builtin_amdgcn_rcpf(1.f + __builtin_amdgcn_exp2f(-1.4426950408889634f * s));
      __hip_atomic_store(wsGr + row, (T & ~0xffffull) | (pk_bf(g, g) & 0xffffu), RLX_AGENT);
    }
    ST(1);
    return;
  }
  const int c0 = 80 * bx + 4 * (bj - 1);
  const int ch = w & 3, br = w >> 2, d = c0 + ch;
  u32 p2[6]; u32 p1[6];
  {
    const int o2 = d * N + 2 * lane, o1 = d * N + 128 + lane;
    if (br == 0) {
      const u16* a[6] = {aAre, aAim, aCre, aCim, aBre, aBim};
#pragma unroll
      for (int i = 0; i < 6; ++i) { p2[i] = *(const u32*)(a[i] + o2); p1[i] = a[i][o1]; }
    } else {
      const u16* a[4] = {bLA, bAim, bCre, bCim};
#pragma unroll
      for (int i = 0; i < 4; ++i) { p2[i] = *(const u32*)(a[i] + o2); p1[i] = a[i][o1]; }
      p2[4] = p2[5] = p1[4] = p1[5] = 0;
    }
  }
  const float ldt = bf_lo((br ? bLdt : aLdt)[d]);
  const float dsk = bf_lo((br ? bD : aD)[d]);
  u64 E = 0;
  if (w == 0) E = __hip_atomic_load(wsE, RLX_AGENT);
  u32x2 ur[4];
#pragma unroll
  for (int i = 0; i < 4; ++i) ur[i] = *(const u32x2*)(u + (size_t)(256 * w + 64 * i + lane) * D + c0);
  const u32 krb = KR_OFF + (2 * ch + br) * KR_ST, ksb = KS_OFF + (2 * ch + br) * KS_ST;
  {
    const u32x4 z = {0, 0, 0, 0};
    if (lane < 56) lds_st<u32x4>(krb + 16 * lane, z);
    lds_st<u32x4>(ksb + 16 * lane, z);
    if (lane < 4) lds_st<u32x4>(ksb + 1024 + 16 * lane, z);
    if (w == 0 && lane < 4) lds_st<u32x4>(R_OFF + 16 * lane, z);
    lds_st<u32>(21 * T_ROW + 256 * w + 4 * lane, 0x00003c00u);
  }
  f32x2 zz[3], ww[3];
  float unscale;
  {
    const float dt = rbf(__builtin_amdgcn_exp2f(1.4426950408889634f * ldt));
    const f32x2 dz = {dt * 1.4426950408889634f, dt * 0.15915494309189535f};
    int emax = -120;
#pragma unroll
    for (int k = 0; k < 3; ++k) {
      float v[6];
#pragma unroll
      for (int i = 0; i < 6; ++i) v[i] = k == 0 ? bf_lo(p2[i]) : k == 1 ? bf_hi(p2[i]) : bf_lo(p1[i]);
      f32x2 cw;
      if (br == 0) {
        cw = (f32x2){v[2] * v[4] - v[3] * v[5], -(v[2] * v[5] + v[3] * v[4])} * dt;
      } else {
        v[0] = -rbf(__builtin_amdgcn_exp2f(1.4426950408889634f * v[0]));
        cw = (f32x2){v[2], -v[3]} * (2.f * dt);
      }
      zz[k] = (f32x2){v[0], v[1]} * dz;
      ww[k] = cw;
      int e = __builtin_amdgcn_frexp_expf(fmaxf(fabsf(cw[0]), fabsf(cw[1])));
      if (br == 0) e += (int)fminf(fmaxf(ceilf(2048.f * zz[k][0]), 0.f), 200.f);
      emax = max(emax, e);
    }
#pragma unroll
    for (int o = 1; o < 64; o <<= 1) emax = max(emax, __shfl_xor(emax, o));
    emax = __builtin_amdgcn_readfirstlane(min(emax, 126));
    const float sc = __int_as_float((127 + 14 - emax > 254 ? 254 : 127 + 14 - emax) << 23);
    unscale = 1.f / sc;
#pragma unroll
    for (int k = 0; k < 3; ++k) ww[k] *= sc;
  }
  if (w == 0) {
    const u64 T = next_tag(E);
    if (lane == 0) { __hip_atomic_store(wsAck + blockIdx.x, T, RLX_AGENT); lds_st<u64>(MISC_OFF, T); }
  }
  ST(1);
  f32x16 acc = {0};
  u32x4 gr0, gr1;
  const u32 fa = (lane & 31) * T_ROW + 256 * w + 16 * (lane >> 5);
#pragma unroll 1
  for (int k = 0; k < 3; ++k) {
    const f32x2 z = zz[0], W = ww[0];
    zz[0] = zz[1]; zz[1] = zz[2]; ww[0] = ww[1]; ww[1] = ww[2];
    const float e = __builtin_amdgcn_exp2f(z[0]), f = __builtin_amdgcn_fractf(z[1]);
    f32x2 m = (f32x2){__builtin_amdgcn_cosf(f), __builtin_amdgcn_sinf(f)} * e;
    h16x2 M[8];
#pragma unroll
    for (int i = 0; i < 8; ++i) { M[i] = pk_h(m); m = csq_f(m); }
    set_m0(256 * w);
    {
      h16x2 Y[8];
      Y[0] = pk_h(W); tw<0>(Y[0]);
      Y[1] = cmulc_h(Y[0], M[0]); tw<1 * T_ROW>(Y[1]);
      Y[2] = cmulc_h(Y[0], M[1]); tw<2 * T_ROW>(Y[2]);
      Y[3] = cmulc_h(Y[1], M[1]); tw<3 * T_ROW>(Y[3]);
      Y[4] = cmulc_h(Y[0], M[2]); tw<4 * T_ROW>(Y[4]);
      Y[5] = cmulc_h(Y[1], M[2]); tw<5 * T_ROW>(Y[5]);
      Y[6] = cmulc_h(Y[2], M[2]); tw<6 * T_ROW>(Y[6]);
      Y[7] = cmulc_h(Y[3], M[2]); tw<7 * T_ROW>(Y[7]);
      f32x2 Z = cmulc_f(W, m);
      h16x2 a = pk_h(Z); tw<8 * T_ROW>(a);
      tw<9 * T_ROW>(cmulc_h(a, M[2]));
      a = cmulc_h(a, M[1]); tw<10 * T_ROW>(a);
      tw<11 * T_ROW>(cmulc_h(a, M[2]));
      Z = cmulc_f(Z, m); a = pk_h(Z); tw<12 * T_ROW>(a);
      tw<13 * T_ROW>(cmulc_h(a, M[2]));
      Z = cmulc_f(Z, m); a = pk_h(Z); tw<14 * T_ROW>(a);
      tw<15 * T_ROW>(cmulc_h(a, M[2]));
      Z = cmulc_f(Z, m); tw<16 * T_ROW>(pk_h(Z));
      Z = cmulc_f(Z, m); tw<17 * T_ROW>(pk_h(Z));
      Z = cmulc_f(Z, m); tw<18 * T_ROW>(pk_h(Z));
      Z = cmulc_f(Z, m); tw<19 * T_ROW>(pk_h(Z));
      Z = cmulc_f(Z, m); tw<20 * T_ROW>(pk_h(Z));
    }
    set_m0(22 * T_ROW + 256 * w);
    {
      h16x2 X[16];
#define COL(c, v) tw<((c) - 1) * T_ROW>(v)
      X[1] = M[3]; COL(1, X[1]);
      X[2] = M[4]; COL(2, X[2]);
      X[3] = cmul_h(X[1], M[4]); COL(3, X[3]);
      X[4] = M[5]; COL(4, X[4]);
      X[5] = cmul_h(X[1], M[5]); COL(5, X[5]);
      X[6] = cmul_h(X[2], M[5]); COL(6, X[6]);
      X[7] = cmul_h(X[3], M[5]); COL(7, X[7]);
      X[8] = M[6]; COL(8, X[8]);
#define C8(c) X[c] = cmul_h(X[(c) - 8], M[6]); COL(c, X[c]);
      C8(9) C8(10) C8(11) C8(12) C8(13) C8(14) C8(15)
      COL(16, M[7]);
#define C16(c) COL(c, cmul_h(X[(c) - 16], M[7]));
      C16(17) C16(18) C16(19) C16(20) C16(21) C16(22) C16(23) C16(24) C16(25) C16(26) C16(27) C16(28) C16(29) C16(30) C16(31)
    }
    if (k == 1) {
      auto rg = __builtin_amdgcn_make_buffer_rsrc((void*)(ws + WS_GR), 0, L * 8, 0x00020000);
      gr0 = __builtin_amdgcn_raw_buffer_load_b128(rg, tid * 16, 0, 16);
      gr1 = __builtin_amdgcn_raw_buffer_load_b128(rg, tid * 16 + 8192, 0, 16);
    }
#pragma unroll
    for (int s = 0; s < 8; ++s)
      acc = __builtin_amdgcn_mfma_f32_32x32x16_f16(lds_ld<h16x8>(fa + 32 * s), lds_ld<h16x8>(fa + 21 * T_ROW + 32 * s), acc, 0, 0, 0);
    asm volatile("" ::: "memory");
  }
  ST(2);
  {
    const int c = lane & 31, h = lane >> 5;
    float k[12];
#pragma unroll
    for (int i = 0; i < 12; ++i) k[i] = acc[i] * unscale;
    if (lane == 0) k[0] += dsk;
    lds_st<u32x2>(krb + 2 * (316 - 8 * c - 4 * h), (u32x2){pk_bf(k[3], k[2]), pk_bf(k[1], k[0])});
    const u32 x0 = pk_bf(k[4], k[5]), x1 = pk_bf(k[6], k[7]), y0 = pk_bf(k[8], k[9]), y1 = pk_bf(k[10], k[11]);
    if (h == 0) {
      if (c > 0) {
        lds_st<u32>(ksb + 2 * (2 * c + 12), x0);
        lds_st<u32>(ksb + 192 + 2 * (2 * c + 12), x1);
      } else {
        lds_st<u32x4>(krb + 2 * 56, (u32x4){x1 & 0xffff0000u, (x0 >> 16) * 0x10001u, (x0 >> 16) | (x1 << 16), (x0 & 0xffffu) * 0x10001u});
        lds_st<u16>(ksb + 384 + 4 * 70 + 2, (u16)y0);
      }
      lds_st<u16>(ksb + 736 + 2 * (8 + c), (u16)y0);
      lds_st<u16>(ksb + 736 + 2 * (40 + c), (u16)(y0 >> 16));
      lds_st<u16>(ksb + 736 + 2 * (72 + c), (u16)y1);
      lds_st<u16>(ksb + 736 + 2 * (104 + c), (u16)(y1 >> 16));
    } else {
      if (c > 0) lds_st<u16>(ksb + 384 + 4 * (c + 6) + 2, (u16)x0);
      else { lds_st<u16>(ksb + 2 * 76, (u16)x0); lds_st<u16>(ksb + 192 + 2 * 76, (u16)x0); lds_st<u16>(ksb + 736 + 2 * 136, (u16)y0); }
      lds_st<u16>(ksb + 384 + 4 * (c + 7), (u16)(x0 >> 16));
      lds_st<u16>(ksb + 384 + 4 * (c + 38) + 2, (u16)x1);
      lds_st<u16>(ksb + 384 + 4 * (c + 39), (u16)(x1 >> 16));
    }
  }
  {
    const int y = 8 + 256 * w + lane;
    const u32 a = R_OFF + 8 * (y ^ (((y >> 5) & 1) << 2));
#pragma unroll
    for (int i = 0; i < 4; ++i) lds_st<u32x2>(a + 512 * i, ur[i]);
  }
  wg_barrier();
  ST(3);
  {
    const u64 T = lds_ld<u64>(MISC_OFF);
    const u32 thi = (u32)(T >> 32), tlo = (u32)T >> 16;
    auto rg = __builtin_amdgcn_make_buffer_rsrc((void*)(ws + WS_GR), 0, L * 8, 0x00020000);
    for (int spin = 0; spin < 200000; ++spin) {
      bool ok = gr0.y == thi && gr0.w == thi && gr1.y == thi && gr1.w == thi &&
                (gr0.x >> 16) == tlo && (gr0.z >> 16) == tlo && (gr1.x >> 16) == tlo && (gr1.z >> 16) == tlo;
      if (__all(ok)) break;
      asm volatile("" ::: "memory");
      gr0 = __builtin_amdgcn_raw_buffer_load_b128(rg, tid * 16, 0, 16);
      gr1 = __builtin_amdgcn_raw_buffer_load_b128(rg, tid * 16 + 8192, 0, 16);
    }
    const int t = 2 * tid;
    const u32 a = G_OFF + 512 * (t >> 7) + 8 * (16 * ((t >> 4) & 3) + 8 * ((t >> 6) & 1) + (t & 7)) + 4 * ((t >> 3) & 1);
    lds_st<u32>(a, gr0.x << 16); lds_st<u32>(a + 8, gr0.z << 16);
    lds_st<u32>(a + 4096, gr1.x << 16); lds_st<u32>(a + 4096 + 8, gr1.z << 16);
  }
  ST(4);
  {
    u32 ra[4];
    const int G = lane >> 4, q = (lane >> 2) & 3, p = lane & 3;
#pragma unroll
    for (int n = 0; n < 4; ++n) { const int y = 32 * G + 8 * p + q + 4 * n; ra[n] = R_OFF + 8 * (y ^ (((y >> 5) & 1) << 2)); }
    const u32 wu = U_OFF + p * U_CH + 160 + 16 * (lane >> 2);
    const u32 wwp = (p << 16) | (160 + 16 * (lane >> 2));
    prep_block<true, 3>(w, ra, wu, wwp);
    if (w < 4) prep_block<true, 2>(w + 8, ra, wu, wwp);
    else if (w < 6) prep_block<true, 1>(w + 8, ra, wu, wwp);
    else prep_block<true, 0>(w + 8, ra, wu, wwp);
    if (tid < 280) {
      const int a = tid / 10, i = tid % 10;
      const u32 base = a < 16 ? U_OFF + (a >> 2) * U_CH + (a & 3) * U_R
                     : a < 20 ? W3_OFF + (a - 16) * W3_CH : a < 24 ? W5_OFF + (a - 20) * W5_CH : W7_OFF + (a - 24) * W7_CH;
      lds_st<u32x4>(base + 16 * i, (u32x4){0, 0, 0, 0});
    }
  }
  wg_barrier();
  ST(5);
  if (w < 4) {
    const int g = lane >> 4, abr = lane & 1, beta = (lane & 15) >> 1;
    const int ap = (lane >> 3) & 1, b = lane & 7, q = (lane >> 2) & 3, p = lane & 3;
    const u32 kr = KR_OFF + (2 * ch + abr) * KR_ST, ks = KS_OFF + (2 * ch + abr) * KS_ST;
    u32x4 A0[10], A1[5], A2[5], A3[8];
#pragma unroll
    for (int s = 0; s < 10; ++s) A0[s] = lds_ld<u32x4>(kr + 2 * (368 - 8 * (g + beta)) - 64 * s);
#pragma unroll
    for (int s = 0; s < 5; ++s) {
      const u32 a = ks + 2 * (4 * g + 2 * beta) + 32 * s;
      A1[s] = (u32x4){lds_ld<u32>(a), lds_ld<u32>(a + 4), lds_ld<u32>(a + 192), lds_ld<u32>(a + 196)};
    }
#pragma unroll
    for (int s = 0; s < 5; ++s) {
      const u32 a = ks + 384 + 4 * (4 * g + beta) + 64 * s;
      const u32 e0 = lds_ld<u32>(a), e1 = lds_ld<u32>(a + 4), e2 = lds_ld<u32>(a + 8), e3 = lds_ld<u32>(a + 12);
      A2[s] = (u32x4){__builtin_amdgcn_perm(e1, e0, 0x05040100), __builtin_amdgcn_perm(e3, e2, 0x05040100),
                      __builtin_amdgcn_perm(e1, e0, 0x07060302), __builtin_amdgcn_perm(e3, e2, 0x07060302)};
    }
    {
      const u32 k1024 = lds_ld<u16>(ks + 736 + 16);
      const int qq = beta - 4 * (g - 2);
      if (g >= 2 && qq >= 0 && qq < 4) { if (qq < 2) A2[4].x = k1024 << (16 * qq); else A2[4].y = k1024 << (16 * (qq - 2)); }
    }
    {
      const int m0 = -7 + 4 * g + beta, odd = m0 & 1;
      const u32 st = odd ? 0x05040504u : 0x05040100u, sc = odd ? 0x07060504u : 0x05040302u;
#pragma unroll
      for (int s = 0; s < 8; ++s) {
        const u32 a = ks + 736 + 2 * (m0 - odd + 8) + 32 * s;
        const u32 e0 = lds_ld<u32>(a), e1 = lds_ld<u32>(a + 4), e2 = lds_ld<u32>(a + 8);
        A3[s] = (u32x4){__builtin_amdgcn_perm(e1, e0, st), __builtin_amdgcn_perm(e2, e1, st),
                        __builtin_amdgcn_perm(e1, e0, sc), __builtin_amdgcn_perm(e2, e1, sc)};
      }
      const int qq = -1 - m0;
      const u32 k0 = qq == 0 ? 0xffff0000u : qq == 1 ? 0x0000ffffu : ~0u, k1 = qq == 2 ? 0xffff0000u : qq == 3 ? 0x0000ffffu : ~0u;
      A3[0].x &= k0; A3[0].z &= k0; A3[0].y &= k1; A3[0].w &= k1;
    }
    const u32 uc = U_OFF + ch * U_CH;
    const u32 b0 = uc + (3 - (b & 3)) * U_R + 2 * (64 * ap + 4 * (b >> 2) + 132 - 8 * g - 96);
    const int xb = 64 * (p >> 1) + 4 * (p & 1);
    const u32 b1w = W3_OFF + ch * W3_CH + 2 * (xb - 208 - 16 * g - 4 * q + 84 + 192);
    const u32 b1u = uc + 2 * U_R + 2 * (xb - 208 - 16 * g - 4 * q + 80 + 192);
    const u32 b2a = W3_OFF + ch * W3_CH + 2 * (xb - 460 - 32 * g - 8 * q + 84 + 512);
    const u32 b2b = W5_OFF + ch * W5_CH + 2 * (xb - 460 - 32 * g - 8 * q + 80 + 512);
    const u32 b2s = g < 2 ? b2a : uc + 3 * U_R + 2 * (xb + 8 * (q + 4 * (g - 2)) - 944 + 1024);
    const u32 b3u = uc + 2 * (xb + 52 - 32 * g - 8 * q + 80);
    const u32 b3w = W7_OFF + ch * W7_CH + 2 * (xb + 52 - 32 * g - 8 * q + 80);
    set_m0(56832 + 256 * ch);
    u32x4 B0[64], B1[28], B2[12], B3[8];
#pragma unroll
    for (int al = 0; al < 16; ++al) {
      f32x4 a0 = {0, 0, 0, 0}, a1 = {0, 0, 0, 0};
#pragma unroll
      for (int i = 0; i < 4; ++i) { const int x = 4 * al + i; B0[x] = cat(lds_ld<u32x2>(b0 + 64 * x), lds_ld<u32x2>(b0 + 64 * x + 8)); }
      if (al >= 2) {
#pragma unroll
        for (int i = 0; i < 2; ++i) { const int x = 2 * (al - 2) + i; B1[x] = cat(lds_tr(b1w + 128 * x), lds_tr(b1u + 128 * x)); }
      }
      if (al >= 4) { const int x = al - 4; B2[x] = cat(lds_tr(b2a + 256 * x), lds_tr(b2b + 256 * x)); }
      if (al >= 8) { const int x = al - 8; B3[x] = cat(lds_tr(b3u + 256 * x), lds_tr(b3w + 256 * x)); }
#pragma unroll
      for (int s = 0; s < 10; ++s) if (s < 4 * al + 4) a0 = mm(A0[s], B0[4 * al + 3 - s], a0);
#pragma unroll
      for (int s = 0; s < 5; ++s) if (s < 2 * al - 2) a1 = mm(A1[s], B1[2 * al - 3 - s], a1);
#pragma unroll
      for (int s = 0; s < 4; ++s) if (s < al - 3) a1 = mm(A2[s], B2[al - 4 - s], a1);
      if (al >= 8) { const int x = al - 8; a1 = mm(A2[4], cat(lds_tr(b2s + 256 * x), lds_tr(b2b + 256 * x)), a1); }
#pragma unroll
      for (int s = 0; s < 8; ++s) if (s < al - 7) a0 = mm(A3[s], B3[al - 8 - s], a0);
      a0 += a1;
      const f32x2 gg = lds_ld<f32x2>(G_OFF + 512 * al + 8 * lane);
      const u32 o = pk_bf(fmaf(gg[0], a0[0] - a0[1], a0[1]), fmaf(gg[1], a0[2] - a0[3], a0[3]));
      asm volatile("ds_write_addtid_b32 %0 offset:%1" ::"v"(o), "i"(49152 + 1024 * al) : "memory");
    }
  }
  ST(6);
  wg_barrier();
  {
    const int t1 = 64 * ((lane >> 3) & 1) + 16 * (lane >> 4) + (lane & 7);
#pragma unroll
    for (int i = 0; i < 2; ++i) {
      const int al = w + 8 * i;
      const u32 a = O_OFF + 1024 * al + 4 * lane;
      const u32 e0 = lds_ld<u32>(a), e1 = lds_ld<u32>(a + 256), e2 = lds_ld<u32>(a + 512), e3 = lds_ld<u32>(a + 768);
      u16* po = out + (size_t)(128 * al + t1) * D + c0;
      auto ro = __builtin_amdgcn_make_buffer_rsrc((void*)out, 0, L * D * 2, 0x00020000);
      const int oo = 2 * ((128 * al + t1) * D + c0);
      __builtin_amdgcn_raw_buffer_store_b64((u32x2){__builtin_amdgcn_perm(e1, e0, 0x05040100), __builtin_amdgcn_perm(e3, e2, 0x05040100)}, ro, oo, 0, 16);
      __builtin_amdgcn_raw_buffer_store_b64((u32x2){__builtin_amdgcn_perm(e1, e0, 0x07060302), __builtin_amdgcn_perm(e3, e2, 0x07060302)}, ro, oo + 16 * D, 0, 16);
    }
  }
  asm volatile(".Lssm_code_b:");
  asm volatile("s_waitcnt vmcnt(0)" ::: "memory");
  while (__builtin_amdgcn_s_memrealtime() - dg_0 < 3000) __builtin_amdgcn_s_sleep(1);

}

extern "C" void kernel_launch(void* const* d_in, const int* in_sizes, int n_in,
                              void* d_out, int out_size, void* d_ws, size_t ws_size,
                              hipStream_t stream) {
  const u16* const* in = (const u16* const*)d_in;
  (void)hipFuncSetAttribute((const void*)ssm_mix, hipFuncAttributeMaxDynamicSharedMemorySize, LDS_BYTES);
  ssm_mix<<<256, 512, LDS_BYTES, stream>>>(in[0], in[1], in[2], in[3], in[4], in[5], in[6], in[7], in[8],
                                           in[10], in[11], in[12], in[13], in[14], in[15], in[16],
                                           (u16*)d_out, (char*)d_ws);
}
```

```cpp
#include <hip/hip_runtime.h>
#include <hip/hip_bf16.h>
#include <stdint.h>
#pragma clang diagnostic ignored "-Wint-to-pointer-cast"

typedef unsigned long long u64;
typedef unsigned u32;
typedef unsigned short u16;
typedef u32 u32x2 __attribute__((ext_vector_type(2)));
typedef u32 u32x4 __attribute__((ext_vector_type(4)));
typedef short s16x4 __attribute__((ext_vector_type(4)));
typedef float f32x2 __attribute__((ext_vector_type(2)));
typedef float f32x4 __attribute__((ext_vector_type(4)));
typedef float f32x16 __attribute__((ext_vector_type(16)));
typedef _Float16 h16x2 __attribute__((ext_vector_type(2)));
typedef _Float16 h16x8 __attribute__((ext_vector_type(8)));
typedef __bf16 b16x2 __attribute__((ext_vector_type(2)));
typedef __bf16 b16x8 __attribute__((ext_vector_type(8)));
#define LAS __attribute__((address_space(3)))
#define GAS __attribute__((address_space(1)))
#define CAS __attribute__((address_space(4)))
#define ENTRY_STAMP
#define RLX_AGENT __ATOMIC_RELAXED, __HIP_MEMORY_SCOPE_AGENT
#define DEV __device__ __forceinline__

constexpr int L = 2048, D = 640, N = 192;
constexpr int U_OFF = 0, U_CH = 17312, U_R = 4320;
constexpr int W3_OFF = 69248, W3_CH = 3744;
constexpr int W5_OFF = 84224, W5_CH = 3232;
constexpr int W7_OFF = 97152, W7_CH = 2208;
constexpr int O_OFF = 105984;
constexpr int T_ROW = 2064;
constexpr int R_OFF = 122368;
constexpr int KR_OFF = 138816, KR_ST = 896;
constexpr int KS_OFF = 145984, KS_ST = 1088;
constexpr int G_OFF = 154688;
constexpr int MISC_OFF = 162880;
constexpr int LDS_BYTES = 163840;
constexpr int WS_ACK = 4096, WS_GR = 8192, WS_STAMP = 1 << 20;

DEV float bf_lo(u32 v) { return __uint_as_float(v << 16); }
DEV float bf_hi(u32 v) { return __uint_as_float(v & 0xffff0000u); }
DEV u32 pk_bf(float a, float b) { f32x2 x = {a, b}; b16x2 y = __builtin_convertvector(x, b16x2); return __builtin_bit_cast(u32, y); }
DEV float rbf(float a) { return bf_lo(pk_bf(a, a)); }
DEV h16x2 pk_h(f32x2 x) { return __builtin_convertvector(x, h16x2); }
DEV h16x2 cmul_h(h16x2 x, h16x2 m) {
  h16x2 t = x * (h16x2){m[0], m[0]};
  return __builtin_elementwise_fma((h16x2){-x[1], x[0]}, (h16x2){m[1], m[1]}, t);
}
DEV h16x2 cmulc_h(h16x2 x, h16x2 m) {
  h16x2 t = x * (h16x2){m[0], m[0]};
  return __builtin_elementwise_fma((h16x2){x[1], -x[0]}, (h16x2){m[1], m[1]}, t);
}
DEV f32x2 cmulc_f(f32x2 x, f32x2 m) {
  f32x2 t = x * (f32x2){m[0], m[0]};
  return __builtin_elementwise_fma((f32x2){x[1], -x[0]}, (f32x2){m[1], m[1]}, t);
}
DEV f32x2 csq_f(f32x2 x) {
  f32x2 t = x * (f32x2){x[0], x[0]};
  return __builtin_elementwise_fma((f32x2){-x[1], x[0]}, (f32x2){x[1], x[1]}, t);
}
template <int OFF> DEV void tw(h16x2 v) {
  asm volatile("ds_write_addtid_b32 %0 offset:%1" ::"v"(v), "i"(OFF) : "memory");
}
DEV void set_m0(u32 v) { asm volatile("s_mov_b32 m0, %0\n\ts_nop 0" ::"s"(v) : "memory"); }
template <typename T> DEV T lds_ld(u32 a) { return *(LAS T*)a; }
template <typename T> DEV void lds_st(u32 a, T v) { *(LAS T*)a = v; }
DEV u32x2 lds_tr(u32 a) { return __builtin_bit_cast(u32x2, __builtin_amdgcn_ds_read_tr16_b64_v4i16((LAS s16x4*)a)); }
DEV u64 next_tag(u64 e) { return e * 6364136223846793005ull + 1442695040888963407ull; }
DEV void wg_barrier() { asm volatile("s_waitcnt lgkmcnt(0)\n\ts_barrier" ::: "memory"); }
DEV f32x4 mm(u32x4 a, u32x4 b, f32x4 c) {
  return __builtin_amdgcn_mfma_f32_16x16x32_bf16(__builtin_bit_cast(b16x8, a), __builtin_bit_cast(b16x8, b), c, 0, 0, 0);
}
DEV u32x4 cat(u32x2 a, u32x2 b) { return (u32x4){a.x, a.y, b.x, b.y}; }
#define ST(i)

template <bool DO_U, int NW> DEV void prep_block(int gam, const u32 (&ra)[4], u32 wu, u32 ww) {
  u32 d[8];
#pragma unroll
  for (int n = 0; n < 4; ++n) { u32x2 v = lds_tr(ra[n] + 1024 * gam); d[2 * n] = v.x; d[2 * n + 1] = v.y; }
  if (DO_U) {
    u32 o[5];
#pragma unroll
    for (int k = 0; k < 5; ++k) o[k] = __builtin_amdgcn_alignbit(d[k + 3], d[k + 2], 16);
    u32 a = wu + 256 * gam;
    lds_st<u32x4>(a, (u32x4){d[4], d[5], d[6], d[7]});
    lds_st<u32x4>(a + U_R, (u32x4){o[1], o[2], o[3], o[4]});
    lds_st<u32x4>(a + 2 * U_R, (u32x4){d[3], d[4], d[5], d[6]});
    lds_st<u32x4>(a + 3 * U_R, (u32x4){o[0], o[1], o[2], o[3]});
  }
  if (NW > 0) {
    float P[16];
    constexpr int k0 = 4 - NW, k1 = 11 + NW;
    P[k0 - 1] = 0.f;
#pragma unroll
    for (int k = k0; k <= k1; ++k) { float v = (k & 1) ? bf_hi(d[k >> 1]) : bf_lo(d[k >> 1]); P[k] = k == k0 ? v : P[k - 1] + v; }
#pragma unroll
    for (int h = 1; h <= NW; ++h) {
      u32 r[4];
#pragma unroll
      for (int j = 0; j < 8; j += 2) r[j >> 1] = pk_bf(P[4 + j + h] - P[3 + j - h], P[5 + j + h] - P[4 + j - h]);
      u32 a = (h == 1 ? W3_OFF + (ww >> 16) * W3_CH : h == 2 ? W5_OFF + (ww >> 16) * W5_CH : W7_OFF + (ww >> 16) * W7_CH) + (ww & 0xffff) + 256 * gam;
      lds_st<u32x4>(a, (u32x4){r[0], r[1], r[2], r[3]});
    }
  }
}

__global__ void __launch_bounds__(512, 2)
ssm_mix(const u16* a0, const u16* a1, const u16* a2, const u16* a3, const u16* a4, const u16* a5, const u16* a6,
        const u16* a7, const u16* a8, const u16* a9, const u16* a10, const u16* a11, const u16* a12, const u16* a13,
        const u16* a14, const u16* a15, u16* a16, char* a17) {
  ENTRY_STAMP
  typedef const GAS u16* gp;
  typedef u64 u64x8 __attribute__((ext_vector_type(8)));
  typedef u64 u64x2 __attribute__((ext_vector_type(2)));
  const CAS char* ka = (const CAS char*)__builtin_amdgcn_kernarg_segment_ptr();
  asm volatile("" : "+s"(ka));
  u64x8 k0 = *(const CAS u64x8*)ka, k1 = *(const CAS u64x8*)(ka + 64);
  u64x2 k2 = *(const CAS u64x2*)(ka + 128);
  asm volatile("" : "+s"(k0), "+s"(k1), "+s"(k2));
  const gp u = (gp)k0[0], aAre = (gp)k0[1], aAim = (gp)k0[2], aBre = (gp)k0[3], aBim = (gp)k0[4], aCre = (gp)k0[5],
           aCim = (gp)k0[6], aLdt = (gp)k0[7], aD = (gp)k1[0], bLA = (gp)k1[1], bAim = (gp)k1[2], bCre = (gp)k1[3],
           bCim = (gp)k1[4], bLdt = (gp)k1[5], bD = (gp)k1[6], gw = (gp)k1[7];
  GAS u16* const out = (GAS u16*)k2[0];
  GAS char* const ws = (GAS char*)k2[1];
  extern __shared__ __attribute__((aligned(16))) char smem[];
  asm volatile(".Lssm_code_0:");
  const int tid = threadIdx.x, lane = tid & 63;
  const int w = __builtin_amdgcn_readfirstlane(tid >> 6);
  const int bx = blockIdx.x & 7, bj = blockIdx.x >> 3;
  GAS u64* wsE = (GAS u64*)ws;
  GAS u64* wsAck = (GAS u64*)(ws + WS_ACK);
  GAS u64* wsGr = (GAS u64*)(ws + WS_GR);
  ST(0);
  if (bj == 0 || bj > 20) {
    if (bj == 0) {
      if (w < 7) {
        u64 pc; int back, len;
        asm volatile("s_getpc_b64 %0\n.Lssm_code_a:\n\ts_mov_b32 %1, .Lssm_code_a-.Lssm_code_0\n\ts_mov_b32 %2, .Lssm_code_b-.Lssm_code_0"
                     : "=s"(pc), "=s"(back), "=s"(len));
        if (tid * 128 < len) {
          u32 v = __builtin_nontemporal_load((const GAS u32*)(pc - back) + tid * 32);
          asm volatile("" ::"v"(v));
        }
        return;
      }
      u64 T = next_tag(__hip_atomic_load(wsE, RLX_AGENT));
      if (lane == 0) __hip_atomic_store(wsAck + blockIdx.x, T, RLX_AGENT);
      if (bx != 0) return;
      for (int spin = 0; spin < 200000; ++spin) {
        __builtin_amdgcn_s_sleep(16);
        bool ok = true;
#pragma unroll
        for (int i = 0; i < 4; ++i) ok &= __hip_atomic_load(wsAck + i * 64 + lane, RLX_AGENT) == T;
        if (__all(ok)) { if (lane == 0) __hip_atomic_store(wsE, T, RLX_AGENT); break; }
      }
      return;
    }
    const int hh = (bj - 21) * 8 + bx;
    float s = 0.f;
    const int row = hh * 24 + w * 4 + (lane >> 4);
    if (w < 6 && hh * 24 + w * 4 < L) {
      const GAS u32x4* pu = (const GAS u32x4*)(u + (size_t)row * D) + (lane & 15);
      const GAS u32x4* pw = (const GAS u32x4*)gw + (lane & 15);
      u32x4 a[5], b[5];
#pragma unroll
      for (int i = 0; i < 5; ++i) { a[i] = pu[16 * i]; b[i] = pw[16 * i]; }
#pragma unroll
      for (int i = 0; i < 5; ++i)
#pragma unroll
        for (int k = 0; k < 4; ++k) {
          const u32 x = a[i][k], y = b[i][k];
          s = __builtin_amdgcn_fdot2_f32_bf16(__builtin_bit_cast(b16x2, x), __builtin_bit_cast(b16x2, y), s, false);
        }
      s += __shfl_xor(s, 1); s += __shfl_xor(s, 2); s += __shfl_xor(s, 4); s += __shfl_xor(s, 8);
    } else if (w == 7) {
      u64 T = next_tag(__hip_atomic_load(wsE, RLX_AGENT));
      if (lane == 0) { __hip_atomic_store(wsAck + blockIdx.x, T, RLX_AGENT); lds_st<u64>(MISC_OFF, T); }
    }
    wg_barrier();
    if (w < 6 && hh * 24 + w * 4 < L && (lane & 15) == 0) {
      u64 T = lds_ld<u64>(MISC_OFF);
      s = rbf(s);
      float g = __builtin_amdgcn_rcpf(1.f + __builtin_amdgcn_exp2f(-1.4426950408889634f * s));
      __hip_atomic_store(wsGr + row, (T & ~0xffffull) | (pk_bf(g, g) & 0xffffu), RLX_AGENT);
    }
    ST(1);
    return;
  }
  const int c0 = 80 * bx + 4 * (bj - 1);
  const int ch = w & 3, br = w >> 2, d = c0 + ch;
  u32 p2[6]; u32 p1[6];
  {
    const int o2 = d * N + 2 * lane, o1 = d * N + 128 + lane;
    if (br == 0) {
      const gp a[6] = {aAre, aAim, aCre, aCim, aBre, aBim};
#pragma unroll
      for (int i = 0; i < 6; ++i) { p2[i] = *(const GAS u32*)(a[i] + o2); p1[i] = a[i][o1]; }
    } else {
      const gp a[4] = {bLA, bAim, bCre, bCim};
#pragma unroll
      for (int i = 0; i < 4; ++i) { p2[i] = *(const GAS u32*)(a[i] + o2); p1[i] = a[i][o1]; }
      p2[4] = p2[5] = p1[4] = p1[5] = 0;
    }
  }
  const float ldt = bf_lo((br ? bLdt : aLdt)[d]);
  const float dsk = bf_lo((br ? bD : aD)[d]);
  u64 E = 0;
  if (w == 0) E = __hip_atomic_load(wsE, RLX_AGENT);
  u32x2 ur[4];
#pragma unroll
  for (int i = 0; i < 4; ++i) ur[i] = *(const GAS u32x2*)(u + (size_t)(256 * w + 64 * i + lane) * D + c0);
  const u32 krb = KR_OFF + (2 * ch + br) * KR_ST, ksb = KS_OFF + (2 * ch + br) * KS_ST;
  {
    const u32x4 z = {0, 0, 0, 0};
    if (lane < 56) lds_st<u32x4>(krb + 16 * lane, z);
    lds_st<u32x4>(ksb + 16 * lane, z);
    if (lane < 4) lds_st<u32x4>(ksb + 1024 + 16 * lane, z);
    if (w == 0 && lane < 4) lds_st<u32x4>(R_OFF + 16 * lane, z);
    lds_st<u32>(21 * T_ROW + 256 * w + 4 * lane, 0x00003c00u);
  }
  f32x2 zz[3], ww[3];
  float unscale;
  {
    const float dt = rbf(__builtin_amdgcn_exp2f(1.4426950408889634f * ldt));
    const f32x2 dz = {dt * 1.4426950408889634f, dt * 0.15915494309189535f};
    int emax = -120;
#pragma unroll
    for (int k = 0; k < 3; ++k) {
      float v[6];
#pragma unroll
      for (int i = 0; i < 6; ++i) v[i] = k == 0 ? bf_lo(p2[i]) : k == 1 ? bf_hi(p2[i]) : bf_lo(p1[i]);
      f32x2 cw;
      if (br == 0) {
        cw = (f32x2){v[2] * v[4] - v[3] * v[5], -(v[2] * v[5] + v[3] * v[4])} * dt;
      } else {
        v[0] = -rbf(__builtin_amdgcn_exp2f(1.4426950408889634f * v[0]));
        cw = (f32x2){v[2], -v[3]} * (2.f * dt);
      }
      zz[k] = (f32x2){v[0], v[1]} * dz;
      ww[k] = cw;
      int e = __builtin_amdgcn_frexp_expf(fmaxf(fabsf(cw[0]), fabsf(cw[1])));
      if (br == 0) e += (int)fminf(fmaxf(ceilf(2048.f * zz[k][0]), 0.f), 200.f);
      emax = max(emax, e);
    }
#pragma unroll
    for (int o = 1; o < 64; o <<= 1) emax = max(emax, __shfl_xor(emax, o));
    emax = __builtin_amdgcn_readfirstlane(min(emax, 126));
    const float sc = __int_as_float((127 + 14 - emax > 254 ? 254 : 127 + 14 - emax) << 23);
    unscale = 1.f / sc;
#pragma unroll
    for (int k = 0; k < 3; ++k) ww[k] *= sc;
  }
  if (w == 0) {
    const u64 T = next_tag(E);
    if (lane == 0) { __hip_atomic_store(wsAck + blockIdx.x, T, RLX_AGENT); lds_st<u64>(MISC_OFF, T); }
  }
  ST(1);
  f32x16 acc = {0};
  u32x4 gr0, gr1;
  const u32 fa = (lane & 31) * T_ROW + 256 * w + 16 * (lane >> 5);
#pragma unroll 1
  for (int k = 0; k < 3; ++k) {
    const f32x2 z = zz[0], W = ww[0];
    zz[0] = zz[1]; zz[1] = zz[2]; ww[0] = ww[1]; ww[1] = ww[2];
    const float e = __builtin_amdgcn_exp2f(z[0]), f = __builtin_amdgcn_fractf(z[1]);
    f32x2 m = (f32x2){__builtin_amdgcn_cosf(f), __builtin_amdgcn_sinf(f)} * e;
    h16x2 M[8];
#pragma unroll
    for (int i = 0; i < 8; ++i) { M[i] = pk_h(m); m = csq_f(m); }
    set_m0(256 * w);
    {
      h16x2 Y[8];
      Y[0] = pk_h(W); tw<0>(Y[0]);
      Y[1] = cmulc_h(Y[0], M[0]); tw<1 * T_ROW>(Y[1]);
      Y[2] = cmulc_h(Y[0], M[1]); tw<2 * T_ROW>(Y[2]);
      Y[3] = cmulc_h(Y[1], M[1]); tw<3 * T_ROW>(Y[3]);
      Y[4] = cmulc_h(Y[0], M[2]); tw<4 * T_ROW>(Y[4]);
      Y[5] = cmulc_h(Y[1], M[2]); tw<5 * T_ROW>(Y[5]);
      Y[6] = cmulc_h(Y[2], M[2]); tw<6 * T_ROW>(Y[6]);
      Y[7] = cmulc_h(Y[3], M[2]); tw<7 * T_ROW>(Y[7]);
      f32x2 Z = cmulc_f(W, m);
      h16x2 a = pk_h(Z); tw<8 * T_ROW>(a);
      tw<9 * T_ROW>(cmulc_h(a, M[2]));
      a = cmulc_h(a, M[1]); tw<10 * T_ROW>(a);
      tw<11 * T_ROW>(cmulc_h(a, M[2]));
      Z = cmulc_f(Z, m); a = pk_h(Z); tw<12 * T_ROW>(a);
      tw<13 * T_ROW>(cmulc_h(a, M[2]));
      Z = cmulc_f(Z, m); a = pk_h(Z); tw<14 * T_ROW>(a);
      tw<15 * T_ROW>(cmulc_h(a, M[2]));
      Z = cmulc_f(Z, m); tw<16 * T_ROW>(pk_h(Z));
      Z = cmulc_f(Z, m); tw<17 * T_ROW>(pk_h(Z));
      Z = cmulc_f(Z, m); tw<18 * T_ROW>(pk_h(Z));
      Z = cmulc_f(Z, m); tw<19 * T_ROW>(pk_h(Z));
      Z = cmulc_f(Z, m); tw<20 * T_ROW>(pk_h(Z));
    }
    set_m0(22 * T_ROW + 256 * w);
    {
      h16x2 X[16];
#define COL(c, v) tw<((c) - 1) * T_ROW>(v)
      X[1] = M[3]; COL(1, X[1]);
      X[2] = M[4]; COL(2, X[2]);
      X[3] = cmul_h(X[1], M[4]); COL(3, X[3]);
      X[4] = M[5]; COL(4, X[4]);
      X[5] = cmul_h(X[1], M[5]); COL(5, X[5]);
      X[6] = cmul_h(X[2], M[5]); COL(6, X[6]);
      X[7] = cmul_h(X[3], M[5]); COL(7, X[7]);
      X[8] = M[6]; COL(8, X[8]);
#define C8(c) X[c] = cmul_h(X[(c) - 8], M[6]); COL(c, X[c]);
      C8(9) C8(10) C8(11) C8(12) C8(13) C8(14) C8(15)
      COL(16, M[7]);
#define C16(c) COL(c, cmul_h(X[(c) - 16], M[7]));
      C16(17) C16(18) C16(19) C16(20) C16(21) C16(22) C16(23) C16(24) C16(25) C16(26) C16(27) C16(28) C16(29) C16(30) C16(31)
    }
    if (k == 1) {
      auto rg = __builtin_amdgcn_make_buffer_rsrc((GAS void*)(ws + WS_GR), 0, L * 8, 0x00020000);
      gr0 = __builtin_amdgcn_raw_buffer_load_b128(rg, tid * 16, 0, 16);
      gr1 = __builtin_amdgcn_raw_buffer_load_b128(rg, tid * 16 + 8192, 0, 16);
    }
#pragma unroll
    for (int s = 0; s < 8; ++s)
      acc = __builtin_amdgcn_mfma_f32_32x32x16_f16(lds_ld<h16x8>(fa + 32 * s), lds_ld<h16x8>(fa + 21 * T_ROW + 32 * s), acc, 0, 0, 0);
    asm volatile("" ::: "memory");
  }
  ST(2);
  {
    const int c = lane & 31, h = lane >> 5;
    float k[12];
#pragma unroll
    for (int i = 0; i < 12; ++i) k[i] = acc[i] * unscale;
    if (lane == 0) k[0] += dsk;
    lds_st<u32x2>(krb + 2 * (316 - 8 * c - 4 * h), (u32x2){pk_bf(k[3], k[2]), pk_bf(k[1], k[0])});
    const u32 x0 = pk_bf(k[4], k[5]), x1 = pk_bf(k[6], k[7]), y0 = pk_bf(k[8], k[9]), y1 = pk_bf(k[10], k[11]);
    if (h == 0) {
      if (c > 0) {
        lds_st<u32>(ksb + 2 * (2 * c + 12), x0);
        lds_st<u32>(ksb + 192 + 2 * (2 * c + 12), x1);
      } else {
        lds_st<u32x4>(krb + 2 * 56, (u32x4){x1 & 0xffff0000u, (x0 >> 16) * 0x10001u, (x0 >> 16) | (x1 << 16), (x0 & 0xffffu) * 0x10001u});
        lds_st<u16>(ksb + 384 + 4 * 70 + 2, (u16)y0);
      }
      lds_st<u16>(ksb + 736 + 2 * (8 + c), (u16)y0);
      lds_st<u16>(ksb + 736 + 2 * (40 + c), (u16)(y0 >> 16));
      lds_st<u16>(ksb + 736 + 2 * (72 + c), (u16)y1);
      lds_st<u16>(ksb + 736 + 2 * (104 + c), (u16)(y1 >> 16));
    } else {
      if (c > 0) lds_st<u16>(ksb + 384 + 4 * (c + 6) + 2, (u16)x0);
      else { lds_st<u16>(ksb + 2 * 76, (u16)x0); lds_st<u16>(ksb + 192 + 2 * 76, (u16)x0); lds_st<u16>(ksb + 736 + 2 * 136, (u16)y0); }
      lds_st<u16>(ksb + 384 + 4 * (c + 7), (u16)(x0 >> 16));
      lds_st<u16>(ksb + 384 + 4 * (c + 38) + 2, (u16)x1);
      lds_st<u16>(ksb + 384 + 4 * (c + 39), (u16)(x1 >> 16));
    }
  }
  {
    const int y = 8 + 256 * w + lane;
    const u32 a = R_OFF + 8 * (y ^ (((y >> 5) & 1) << 2));
#pragma unroll
    for (int i = 0; i < 4; ++i) lds_st<u32x2>(a + 512 * i, ur[i]);
  }
  wg_barrier();
  ST(3);
  {
    const u64 T = lds_ld<u64>(MISC_OFF);
    const u32 thi = (u32)(T >> 32), tlo = (u32)T >> 16;
    auto rg = __builtin_amdgcn_make_buffer_rsrc((GAS void*)(ws + WS_GR), 0, L * 8, 0x00020000);
    for (int spin = 0; spin < 200000; ++spin) {
      bool ok = gr0.y == thi && gr0.w == thi && gr1.y == thi && gr1.w == thi &&
                (gr0.x >> 16) == tlo && (gr0.z >> 16) == tlo && (gr1.x >> 16) == tlo && (gr1.z >> 16) == tlo;
      if (__all(ok)) break;
      asm volatile("" ::: "memory");
      gr0 = __builtin_amdgcn_raw_buffer_load_b128(rg, tid * 16, 0, 16);
      gr1 = __builtin_amdgcn_raw_buffer_load_b128(rg, tid * 16 + 8192, 0, 16);
    }
    const int t = 2 * tid;
    const u32 a = G_OFF + 512 * (t >> 7) + 8 * (16 * ((t >> 4) & 3) + 8 * ((t >> 6) & 1) + (t & 7)) + 4 * ((t >> 3) & 1);
    lds_st<u32>(a, gr0.x << 16); lds_st<u32>(a + 8, gr0.z << 16);
    lds_st<u32>(a + 4096, gr1.x << 16); lds_st<u32>(a + 4096 + 8, gr1.z << 16);
  }
  ST(4);
  {
    u32 ra[4];
    const int G = lane >> 4, q = (lane >> 2) & 3, p = lane & 3;
#pragma unroll
    for (int n = 0; n < 4; ++n) { const int y = 32 * G + 8 * p + q + 4 * n; ra[n] = R_OFF + 8 * (y ^ (((y >> 5) & 1) << 2)); }
    const u32 wu = U_OFF + p * U_CH + 160 + 16 * (lane >> 2);
    const u32 wwp = (p << 16) | (160 + 16 * (lane >> 2));
    prep_block<true, 3>(w, ra, wu, wwp);
    if (w < 4) prep_block<true, 2>(w + 8, ra, wu, wwp);
    else if (w < 6) prep_block<true, 1>(w + 8, ra, wu, wwp);
    else prep_block<true, 0>(w + 8, ra, wu, wwp);
    if (tid < 280) {
      const int a = tid / 10, i = tid % 10;
      const u32 base = a < 16 ? U_OFF + (a >> 2) * U_CH + (a & 3) * U_R
                     : a < 20 ? W3_OFF + (a - 16) * W3_CH : a < 24 ? W5_OFF + (a - 20) * W5_CH : W7_OFF + (a - 24) * W7_CH;
      lds_st<u32x4>(base + 16 * i, (u32x4){0, 0, 0, 0});
    }
  }
  wg_barrier();
  ST(5);
  if (w < 4) {
    const int g = lane >> 4, abr = lane & 1, beta = (lane & 15) >> 1;
    const int ap = (lane >> 3) & 1, b = lane & 7, q = (lane >> 2) & 3, p = lane & 3;
    const u32 kr = KR_OFF + (2 * ch + abr) * KR_ST, ks = KS_OFF + (2 * ch + abr) * KS_ST;
    u32x4 A0[10], A1[5], A2[5], A3[8];
#pragma unroll
    for (int s = 0; s < 10; ++s) A0[s] = lds_ld<u32x4>(kr + 2 * (368 - 8 * (g + beta)) - 64 * s);
#pragma unroll
    for (int s = 0; s < 5; ++s) {
      const u32 a = ks + 2 * (4 * g + 2 * beta) + 32 * s;
      A1[s] = (u32x4){lds_ld<u32>(a), lds_ld<u32>(a + 4), lds_ld<u32>(a + 192), lds_ld<u32>(a + 196)};
    }
#pragma unroll
    for (int s = 0; s < 5; ++s) {
      const u32 a = ks + 384 + 4 * (4 * g + beta) + 64 * s;
      const u32 e0 = lds_ld<u32>(a), e1 = lds_ld<u32>(a + 4), e2 = lds_ld<u32>(a + 8), e3 = lds_ld<u32>(a + 12);
      A2[s] = (u32x4){__builtin_amdgcn_perm(e1, e0, 0x05040100), __builtin_amdgcn_perm(e3, e2, 0x05040100),
                      __builtin_amdgcn_perm(e1, e0, 0x07060302), __builtin_amdgcn_perm(e3, e2, 0x07060302)};
    }
    {
      const u32 k1024 = lds_ld<u16>(ks + 736 + 16);
      const int qq = beta - 4 * (g - 2);
      if (g >= 2 && qq >= 0 && qq < 4) { if (qq < 2) A2[4].x = k1024 << (16 * qq); else A2[4].y = k1024 << (16 * (qq - 2)); }
    }
    {
      const int m0 = -7 + 4 * g + beta, odd = m0 & 1;
      const u32 st = odd ? 0x05040504u : 0x05040100u, sc = odd ? 0x07060504u : 0x05040302u;
#pragma unroll
      for (int s = 0; s < 8; ++s) {
        const u32 a = ks + 736 + 2 * (m0 - odd + 8) + 32 * s;
        const u32 e0 = lds_ld<u32>(a), e1 = lds_ld<u32>(a + 4), e2 = lds_ld<u32>(a + 8);
        A3[s] = (u32x4){__builtin_amdgcn_perm(e1, e0, st), __builtin_amdgcn_perm(e2, e1, st),
                        __builtin_amdgcn_perm(e1, e0, sc), __builtin_amdgcn_perm(e2, e1, sc)};
      }
      const int qq = -1 - m0;
      const u32 k0 = qq == 0 ? 0xffff0000u : qq == 1 ? 0x0000ffffu : ~0u, k1 = qq == 2 ? 0xffff0000u : qq == 3 ? 0x0000ffffu : ~0u;
      A3[0].x &= k0; A3[0].z &= k0; A3[0].y &= k1; A3[0].w &= k1;
    }
    const u32 uc = U_OFF + ch * U_CH;
    const u32 b0 = uc + (3 - (b & 3)) * U_R + 2 * (64 * ap + 4 * (b >> 2) + 132 - 8 * g - 96);
    const int xb = 64 * (p >> 1) + 4 * (p & 1);
    const u32 b1w = W3_OFF + ch * W3_CH + 2 * (xb - 208 - 16 * g - 4 * q + 84 + 192);
    const u32 b1u = uc + 2 * U_R + 2 * (xb - 208 - 16 * g - 4 * q + 80 + 192);
    const u32 b2a = W3_OFF + ch * W3_CH + 2 * (xb - 460 - 32 * g - 8 * q + 84 + 512);
    const u32 b2b = W5_OFF + ch * W5_CH + 2 * (xb - 460 - 32 * g - 8 * q + 80 + 512);
    const u32 b2s = g < 2 ? b2a : uc + 3 * U_R + 2 * (xb + 8 * (q + 4 * (g - 2)) - 944 + 1024);
    const u32 b3u = uc + 2 * (xb + 52 - 32 * g - 8 * q + 80);
    const u32 b3w = W7_OFF + ch * W7_CH + 2 * (xb + 52 - 32 * g - 8 * q + 80);
    set_m0(56832 + 256 * ch);
    u32x4 B0[64], B1[28], B2[12], B3[8];
#pragma unroll
    for (int al = 0; al < 16; ++al) {
      f32x4 a0 = {0, 0, 0, 0}, a1 = {0, 0, 0, 0};
#pragma unroll
      for (int i = 0; i < 4; ++i) { const int x = 4 * al + i; B0[x] = cat(lds_ld<u32x2>(b0 + 64 * x), lds_ld<u32x2>(b0 + 64 * x + 8)); }
      if (al >= 2) {
#pragma unroll
        for (int i = 0; i < 2; ++i) { const int x = 2 * (al - 2) + i; B1[x] = cat(lds_tr(b1w + 128 * x), lds_tr(b1u + 128 * x)); }
      }
      if (al >= 4) { const int x = al - 4; B2[x] = cat(lds_tr(b2a + 256 * x), lds_tr(b2b + 256 * x)); }
      if (al >= 8) { const int x = al - 8; B3[x] = cat(lds_tr(b3u + 256 * x), lds_tr(b3w + 256 * x)); }
#pragma unroll
      for (int s = 0; s < 10; ++s) if (s < 4 * al + 4) a0 = mm(A0[s], B0[4 * al + 3 - s], a0);
#pragma unroll
      for (int s = 0; s < 5; ++s) if (s < 2 * al - 2) a1 = mm(A1[s], B1[2 * al - 3 - s], a1);
#pragma unroll
      for (int s = 0; s < 4; ++s) if (s < al - 3) a1 = mm(A2[s], B2[al - 4 - s], a1);
      if (al >= 8) { const int x = al - 8; a1 = mm(A2[4], cat(lds_tr(b2s + 256 * x), lds_tr(b2b + 256 * x)), a1); }
#pragma unroll
      for (int s = 0; s < 8; ++s) if (s < al - 7) a0 = mm(A3[s], B3[al - 8 - s], a0);
      a0 += a1;
      const f32x2 gg = lds_ld<f32x2>(G_OFF + 512 * al + 8 * lane);
      const u32 o = pk_bf(fmaf(gg[0], a0[0] - a0[1], a0[1]), fmaf(gg[1], a0[2] - a0[3], a0[3]));
      asm volatile("ds_write_addtid_b32 %0 offset:%1" ::"v"(o), "i"(49152 + 1024 * al) : "memory");
    }
  }
  ST(6);
  wg_barrier();
  {
    const int t1 = 64 * ((lane >> 3) & 1) + 16 * (lane >> 4) + (lane & 7);
#pragma unroll
    for (int i = 0; i < 2; ++i) {
      const int al = w + 8 * i;
      const u32 a = O_OFF + 1024 * al + 4 * lane;
      const u32 e0 = lds_ld<u32>(a), e1 = lds_ld<u32>(a + 256), e2 = lds_ld<u32>(a + 512), e3 = lds_ld<u32>(a + 768);
      GAS u16* po = out + (size_t)(128 * al + t1) * D + c0;
      *(GAS u32x2*)po = (u32x2){__builtin_amdgcn_perm(e1, e0, 0x05040100), __builtin_amdgcn_perm(e3, e2, 0x05040100)};
      *(GAS u32x2*)(po + 8 * D) = (u32x2){__builtin_amdgcn_perm(e1, e0, 0x07060302), __builtin_amdgcn_perm(e3, e2, 0x07060302)};
    }
  }
  asm volatile(".Lssm_code_b:");
  ST(7);
}

extern "C" void kernel_launch(void* const* d_in, const int* in_sizes, int n_in,
                              void* d_out, int out_size, void* d_ws, size_t ws_size,
                              hipStream_t stream) {
  const u16* const* in = (const u16* const*)d_in;
  (void)hipFuncSetAttribute((const void*)ssm_mix, hipFuncAttributeMaxDynamicSharedMemorySize, LDS_BYTES);
  ssm_mix<<<256, 512, LDS_BYTES, stream>>>(in[0], in[1], in[2], in[3], in[4], in[5], in[6], in[7], in[8],
                                           in[10], in[11], in[12], in[13], in[14], in[15], in[16],
                                           (u16*)d_out, (char*)d_ws);
}
```

```cpp
#include <hip/hip_runtime.h>
#include <hip/hip_bf16.h>
#include <stdint.h>
#pragma clang diagnostic ignored "-Wint-to-pointer-cast"

typedef unsigned long long u64;
typedef unsigned u32;
typedef unsigned short u16;
typedef u32 u32x2 __attribute__((ext_vector_type(2)));
typedef u32 u32x4 __attribute__((ext_vector_type(4)));
typedef short s16x4 __attribute__((ext_vector_type(4)));
typedef float f32x2 __attribute__((ext_vector_type(2)));
typedef float f32x4 __attribute__((ext_vector_type(4)));
typedef float f32x16 __attribute__((ext_vector_type(16)));
typedef _Float16 h16x2 __attribute__((ext_vector_type(2)));
typedef _Float16 h16x8 __attribute__((ext_vector_type(8)));
typedef __bf16 b16x2 __attribute__((ext_vector_type(2)));
typedef __bf16 b16x8 __attribute__((ext_vector_type(8)));
#define LAS __attribute__((address_space(3)))
#define GAS __attribute__((address_space(1)))
#define CAS __attribute__((address_space(4)))
#define ENTRY_STAMP
#define RLX_AGENT __ATOMIC_RELAXED, __HIP_MEMORY_SCOPE_AGENT
#define DEV __device__ __forceinline__

constexpr int L = 2048, D = 640, N = 192;
constexpr int U_OFF = 0, U_CH = 17312, U_R = 4320;
constexpr int W3_OFF = 69248, W3_CH = 3744;
constexpr int W5_OFF = 84224, W5_CH = 3232;
constexpr int W7_OFF = 97152, W7_CH = 2208;
constexpr int O_OFF = 105984;
constexpr int T_ROW = 2064;
constexpr int R_OFF = 122368;
constexpr int KR_OFF = 138816, KR_ST = 896;
constexpr int KS_OFF = 145984, KS_ST = 1088;
constexpr int G_OFF = 154688;
constexpr int MISC_OFF = 162880;
constexpr int LDS_BYTES = 163840;
constexpr int WS_ACK = 4096, WS_GR = 8192, WS_STAMP = 1 << 20;

DEV float bf_lo(u32 v) { return __uint_as_float(v << 16); }
DEV float bf_hi(u32 v) { return __uint_as_float(v & 0xffff0000u); }
DEV u32 pk_bf(float a, float b) { f32x2 x = {a, b}; b16x2 y = __builtin_convertvector(x, b16x2); return __builtin_bit_cast(u32, y); }
DEV float rbf(float a) { return bf_lo(pk_bf(a, a)); }
DEV h16x2 pk_h(f32x2 x) { return __builtin_convertvector(x, h16x2); }
DEV h16x2 cmul_h(h16x2 x, h16x2 m) {
  h16x2 t = x * (h16x2){m[0], m[0]};
  return __builtin_elementwise_fma((h16x2){-x[1], x[0]}, (h16x2){m[1], m[1]}, t);
}
DEV h16x2 cmulc_h(h16x2 x, h16x2 m) {
  h16x2 t = x * (h16x2){m[0], m[0]};
  return __builtin_elementwise_fma((h16x2){x[1], -x[0]}, (h16x2){m[1], m[1]}, t);
}
DEV f32x2 cmulc_f(f32x2 x, f32x2 m) {
  f32x2 t = x * (f32x2){m[0], m[0]};
  return __builtin_elementwise_fma((f32x2){x[1], -x[0]}, (f32x2){m[1], m[1]}, t);
}
DEV f32x2 csq_f(f32x2 x) {
  f32x2 t = x * (f32x2){x[0], x[0]};
  return __builtin_elementwise_fma((f32x2){-x[1], x[0]}, (f32x2){x[1], x[1]}, t);
}
template <int OFF> DEV void tw(h16x2 v) {
  asm volatile("ds_write_addtid_b32 %0 offset:%1" ::"v"(v), "i"(OFF) : "memory");
}
DEV void set_m0(u32 v) { asm volatile("s_mov_b32 m0, %0\n\ts_nop 0" ::"s"(v) : "memory"); }
template <typename T> DEV T lds_ld(u32 a) { return *(LAS T*)a; }
template <typename T> DEV void lds_st(u32 a, T v) { *(LAS T*)a = v; }
DEV u32x2 lds_tr(u32 a) { return __builtin_bit_cast(u32x2, __builtin_amdgcn_ds_read_tr16_b64_v4i16((LAS s16x4*)a)); }
DEV u64 next_tag(u64 e) { return e * 6364136223846793005ull + 1442695040888963407ull; }
DEV void wg_barrier() { asm volatile("s_waitcnt lgkmcnt(0)\n\ts_barrier" ::: "memory"); }
DEV f32x4 mm(u32x4 a, u32x4 b, f32x4 c) {
  return __builtin_amdgcn_mfma_f32_16x16x32_bf16(__builtin_bit_cast(b16x8, a), __builtin_bit_cast(b16x8, b), c, 0, 0, 0);
}
DEV u32x4 cat(u32x2 a, u32x2 b) { return (u32x4){a.x, a.y, b.x, b.y}; }
#define ST(i)

template <bool DO_U, int NW> DEV void prep_block(int gam, const u32 (&ra)[4], u32 wu, u32 ww) {
  u32 d[8];
#pragma unroll
  for (int n = 0; n < 4; ++n) { u32x2 v = lds_tr(ra[n] + 1024 * gam); d[2 * n] = v.x; d[2 * n + 1] = v.y; }
  if (DO_U) {
    u32 o[5];
#pragma unroll
    for (int k = 0; k < 5; ++k) o[k] = __builtin_amdgcn_alignbit(d[k + 3], d[k + 2], 16);
    u32 a = wu + 256 * gam;
    lds_st<u32x4>(a, (u32x4){d[4], d[5], d[6], d[7]});
    lds_st<u32x4>(a + U_R, (u32x4){o[1], o[2], o[3], o[4]});
    lds_st<u32x4>(a + 2 * U_R, (u32x4){d[3], d[4], d[5], d[6]});
    lds_st<u32x4>(a + 3 * U_R, (u32x4){o[0], o[1], o[2], o[3]});
  }
  if (NW > 0) {
    float P[16];
    constexpr int k0 = 4 - NW, k1 = 11 + NW;
    P[k0 - 1] = 0.f;
#pragma unroll
    for (int k = k0; k <= k1; ++k) { float v = (k & 1) ? bf_hi(d[k >> 1]) : bf_lo(d[k >> 1]); P[k] = k == k0 ? v : P[k - 1] + v; }
#pragma unroll
    for (int h = 1; h <= NW; ++h) {
      u32 r[4];
#pragma unroll
      for (int j = 0; j < 8; j += 2) r[j >> 1] = pk_bf(P[4 + j + h] - P[3 + j - h], P[5 + j + h] - P[4 + j - h]);
      u32 a = (h == 1 ? W3_OFF + (ww >> 16) * W3_CH : h == 2 ? W5_OFF + (ww >> 16) * W5_CH : W7_OFF + (ww >> 16) * W7_CH) + (ww & 0xffff) + 256 * gam;
      lds_st<u32x4>(a, (u32x4){r[0], r[1], r[2], r[3]});
    }
  }
}

__global__ void __launch_bounds__(512, 2)
ssm_mix(const u16* a0, const u16* a1, const u16* a2, const u16* a3, const u16* a4, const u16* a5, const u16* a6,
        const u16* a7, const u16* a8, const u16* a9, const u16* a10, const u16* a11, const u16* a12, const u16* a13,
        const u16* a14, const u16* a15, u16* a16, char* a17) {
  ENTRY_STAMP
  typedef const GAS u16* gp;
  typedef u64 u64x8 __attribute__((ext_vector_type(8)));
  typedef u64 u64x2 __attribute__((ext_vector_type(2)));
  const CAS char* ka = (const CAS char*)__builtin_amdgcn_kernarg_segment_ptr();
  asm volatile("" : "+s"(ka));
  u64x8 k0 = *(const CAS u64x8*)ka, k1 = *(const CAS u64x8*)(ka + 64);
  u64x2 k2 = *(const CAS u64x2*)(ka + 128);
  asm volatile("" : "+s"(k0), "+s"(k1), "+s"(k2));
  const gp u = (gp)k0[0], aAre = (gp)k0[1], aAim = (gp)k0[2], aBre = (gp)k0[3], aBim = (gp)k0[4], aCre = (gp)k0[5],
           aCim = (gp)k0[6], aLdt = (gp)k0[7], aD = (gp)k1[0], bLA = (gp)k1[1], bAim = (gp)k1[2], bCre = (gp)k1[3],
           bCim = (gp)k1[4], bLdt = (gp)k1[5], bD = (gp)k1[6], gw = (gp)k1[7];
  GAS u16* const out = (GAS u16*)k2[0];
  GAS char* const ws = (GAS char*)k2[1];
  extern __shared__ __attribute__((aligned(16))) char smem[];
  asm volatile(".Lssm_code_0:");
  const int tid = threadIdx.x, lane = tid & 63;
  const int w = __builtin_amdgcn_readfirstlane(tid >> 6);
  const int bx = blockIdx.x & 7, bj = blockIdx.x >> 3;
  GAS u64* wsE = (GAS u64*)ws;
  GAS u64* wsAck = (GAS u64*)(ws + WS_ACK);
  GAS u64* wsGr = (GAS u64*)(ws + WS_GR);
  ST(0);
  if (bj == 0 || bj > 20) {
    if (bj == 0) {
      if (w < 7) {
        u64 pc; int back, len;
        asm volatile("s_getpc_b64 %0\n.Lssm_code_a:\n\ts_mov_b32 %1, .Lssm_code_a-.Lssm_code_0\n\ts_mov_b32 %2, .Lssm_code_b-.Lssm_code_0"
                     : "=s"(pc), "=s"(back), "=s"(len));
        if (tid * 128 < len) {
          u32 v = __builtin_nontemporal_load((const GAS u32*)(pc - back) + tid * 32);
          asm volatile("" ::"v"(v));
        }
        return;
      }
      u64 T = next_tag(__hip_atomic_load(wsE, RLX_AGENT));
      if (lane == 0) __hip_atomic_store(wsAck + blockIdx.x, T, RLX_AGENT);
      if (bx != 0) return;
      for (int spin = 0; spin < 200000; ++spin) {
        __builtin_amdgcn_s_sleep(16);
        bool ok = true;
#pragma unroll
        for (int i = 0; i < 4; ++i) ok &= __hip_atomic_load(wsAck + i * 64 + lane, RLX_AGENT) == T;
        if (__all(ok)) { if (lane == 0) __hip_atomic_store(wsE, T, RLX_AGENT); break; }
      }
      return;
    }
    const int hh = (bj - 21) * 8 + bx;
    float s = 0.f;
    const int row = hh * 24 + w * 4 + (lane >> 4);
    if (w < 6 && hh * 24 + w * 4 < L) {
      const GAS u32x4* pu = (const GAS u32x4*)(u + (size_t)row * D) + (lane & 15);
      const GAS u32x4* pw = (const GAS u32x4*)gw + (lane & 15);
      u32x4 a[5], b[5];
#pragma unroll
      for (int i = 0; i < 5; ++i) { a[i] = pu[16 * i]; b[i] = pw[16 * i]; }
#pragma unroll
      for (int i = 0; i < 5; ++i)
#pragma unroll
        for (int k = 0; k < 4; ++k) {
          const u32 x = a[i][k], y = b[i][k];
          s = __builtin_amdgcn_fdot2_f32_bf16(__builtin_bit_cast(b16x2, x), __builtin_bit_cast(b16x2, y), s, false);
        }
      s += __shfl_xor(s, 1); s += __shfl_xor(s, 2); s += __shfl_xor(s, 4); s += __shfl_xor(s, 8);
    } else if (w == 7) {
      u64 T = next_tag(__hip_atomic_load(wsE, RLX_AGENT));
      if (lane == 0) { __hip_atomic_store(wsAck + blockIdx.x, T, RLX_AGENT); lds_st<u64>(MISC_OFF, T); }
    }
    wg_barrier();
    if (w < 6 && hh * 24 + w * 4 < L && (lane & 15) == 0) {
      u64 T = lds_ld<u64>(MISC_OFF);
      s = rbf(s);
      float g = __builtin_amdgcn_rcpf(1.f + __builtin_amdgcn_exp2f(-1.4426950408889634f * s));
      __hip_atomic_store(wsGr + row, (T & ~0xffffull) | (pk_bf(g, g) & 0xffffu), RLX_AGENT);
    }
    ST(1);
    return;
  }
  const int c0 = 80 * bx + 4 * (bj - 1);
  const int ch = w & 3, br = w >> 2, d = c0 + ch;
  u32 p2[6]; u32 p1[6];
  {
    const int o2 = d * N + 2 * lane, o1 = d * N + 128 + lane;
    if (br == 0) {
      const gp a[6] = {aAre, aAim, aCre, aCim, aBre, aBim};
#pragma unroll
      for (int i = 0; i < 6; ++i) { p2[i] = *(const GAS u32*)(a[i] + o2); p1[i] = a[i][o1]; }
    } else {
      const gp a[4] = {bLA, bAim, bCre, bCim};
#pragma unroll
      for (int i = 0; i < 4; ++i) { p2[i] = *(const GAS u32*)(a[i] + o2); p1[i] = a[i][o1]; }
      p2[4] = p2[5] = p1[4] = p1[5] = 0;
    }
  }
  const float ldt = bf_lo((br ? bLdt : aLdt)[d]);
  const float dsk = bf_lo((br ? bD : aD)[d]);
  u64 E = 0;
  if (w == 0) E = __hip_atomic_load(wsE, RLX_AGENT);
  u32x2 ur[4];
#pragma unroll
  for (int i = 0; i < 4; ++i) ur[i] = *(const GAS u32x2*)(u + (size_t)(256 * w + 64 * i + lane) * D + c0);
  const u32 krb = KR_OFF + (2 * ch + br) * KR_ST, ksb = KS_OFF + (2 * ch + br) * KS_ST;
  {
    const u32x4 z = {0, 0, 0, 0};
    if (lane < 56) lds_st<u32x4>(krb + 16 * lane, z);
    lds_st<u32x4>(ksb + 16 * lane, z);
    if (lane < 4) lds_st<u32x4>(ksb + 1024 + 16 * lane, z);
    if (w == 0 && lane < 4) lds_st<u32x4>(R_OFF + 16 * lane, z);
    lds_st<u32>(21 * T_ROW + 256 * w + 4 * lane, 0x00003c00u);
  }
  f32x2 zz[3], ww[3];
  float unscale;
  {
    const float dt = rbf(__builtin_amdgcn_exp2f(1.4426950408889634f * ldt));
    const f32x2 dz = {dt * 1.4426950408889634f, dt * 0.15915494309189535f};
    int emax = -120;
#pragma unroll
    for (int k = 0; k < 3; ++k) {
      float v[6];
#pragma unroll
      for (int i = 0; i < 6; ++i) v[i] = k == 0 ? bf_lo(p2[i]) : k == 1 ? bf_hi(p2[i]) : bf_lo(p1[i]);
      f32x2 cw;
      if (br == 0) {
        cw = (f32x2){v[2] * v[4] - v[3] * v[5], -(v[2] * v[5] + v[3] * v[4])} * dt;
      } else {
        v[0] = -rbf(__builtin_amdgcn_exp2f(1.4426950408889634f * v[0]));
        cw = (f32x2){v[2], -v[3]} * (2.f * dt);
      }
      zz[k] = (f32x2){v[0], v[1]} * dz;
      ww[k] = cw;
      int e = __builtin_amdgcn_frexp_expf(fmaxf(fabsf(cw[0]), fabsf(cw[1])));
      if (br == 0) e += (int)fminf(fmaxf(ceilf(2048.f * zz[k][0]), 0.f), 200.f);
      emax = max(emax, e);
    }
#pragma unroll
    for (int o = 1; o < 64; o <<= 1) emax = max(emax, __shfl_xor(emax, o));
    emax = __builtin_amdgcn_readfirstlane(min(emax, 126));
    const float sc = __int_as_float((127 + 14 - emax > 254 ? 254 : 127 + 14 - emax) << 23);
    unscale = 1.f / sc;
#pragma unroll
    for (int k = 0; k < 3; ++k) ww[k] *= sc;
  }
  if (w == 0) {
    const u64 T = next_tag(E);
    if (lane == 0) { __hip_atomic_store(wsAck + blockIdx.x, T, RLX_AGENT); lds_st<u64>(MISC_OFF, T); }
  }
  ST(1);
  f32x16 acc = {0};
  u32x4 gr0, gr1;
  const u32 fa = (lane & 31) * T_ROW + 256 * w + 16 * (lane >> 5);
#pragma unroll 1
  for (int k = 0; k < 3; ++k) {
    const f32x2 z = zz[0], W = ww[0];
    zz[0] = zz[1]; zz[1] = zz[2]; ww[0] = ww[1]; ww[1] = ww[2];
    const float e = __builtin_amdgcn_exp2f(z[0]), f = __builtin_amdgcn_fractf(z[1]);
    f32x2 m = (f32x2){__builtin_amdgcn_cosf(f), __builtin_amdgcn_sinf(f)} * e;
    h16x2 M[8];
#pragma unroll
    for (int i = 0; i < 8; ++i) { M[i] = pk_h(m); m = csq_f(m); }
    set_m0(256 * w);
    {
      h16x2 Y[8];
      Y[0] = pk_h(W); tw<0>(Y[0]);
      Y[1] = cmulc_h(Y[0], M[0]); tw<1 * T_ROW>(Y[1]);
      Y[2] = cmulc_h(Y[0], M[1]); tw<2 * T_ROW>(Y[2]);
      Y[3] = cmulc_h(Y[1], M[1]); tw<3 * T_ROW>(Y[3]);
      Y[4] = cmulc_h(Y[0], M[2]); tw<4 * T_ROW>(Y[4]);
      Y[5] = cmulc_h(Y[1], M[2]); tw<5 * T_ROW>(Y[5]);
      Y[6] = cmulc_h(Y[2], M[2]); tw<6 * T_ROW>(Y[6]);
      Y[7] = cmulc_h(Y[3], M[2]); tw<7 * T_ROW>(Y[7]);
      f32x2 Z = cmulc_f(W, m);
      h16x2 a = pk_h(Z); tw<8 * T_ROW>(a);
      tw<9 * T_ROW>(cmulc_h(a, M[2]));
      a = cmulc_h(a, M[1]); tw<10 * T_ROW>(a);
      tw<11 * T_ROW>(cmulc_h(a, M[2]));
      Z = cmulc_f(Z, m); a = pk_h(Z); tw<12 * T_ROW>(a);
      tw<13 * T_ROW>(cmulc_h(a, M[2]));
      Z = cmulc_f(Z, m); a = pk_h(Z); tw<14 * T_ROW>(a);
      tw<15 * T_ROW>(cmulc_h(a, M[2]));
      Z = cmulc_f(Z, m); tw<16 * T_ROW>(pk_h(Z));
      Z = cmulc_f(Z, m); tw<17 * T_ROW>(pk_h(Z));
      Z = cmulc_f(Z, m); tw<18 * T_ROW>(pk_h(Z));
      Z = cmulc_f(Z, m); tw<19 * T_ROW>(pk_h(Z));
      Z = cmulc_f(Z, m); tw<20 * T_ROW>(pk_h(Z));
    }
    set_m0(22 * T_ROW + 256 * w);
    {
      h16x2 X[16];
#define COL(c, v) tw<((c) - 1) * T_ROW>(v)
      X[1] = M[3]; COL(1, X[1]);
      X[2] = M[4]; COL(2, X[2]);
      X[3] = cmul_h(X[1], M[4]); COL(3, X[3]);
      X[4] = M[5]; COL(4, X[4]);
      X[5] = cmul_h(X[1], M[5]); COL(5, X[5]);
      X[6] = cmul_h(X[2], M[5]); COL(6, X[6]);
      X[7] = cmul_h(X[3], M[5]); COL(7, X[7]);
      X[8] = M[6]; COL(8, X[8]);
#define C8(c) X[c] = cmul_h(X[(c) - 8], M[6]); COL(c, X[c]);
      C8(9) C8(10) C8(11) C8(12) C8(13) C8(14) C8(15)
      COL(16, M[7]);
#define C16(c) COL(c, cmul_h(X[(c) - 16], M[7]));
      C16(17) C16(18) C16(19) C16(20) C16(21) C16(22) C16(23) C16(24) C16(25) C16(26) C16(27) C16(28) C16(29) C16(30) C16(31)
    }
    if (k == 1) {
      auto rg = __builtin_amdgcn_make_buffer_rsrc((GAS void*)(ws + WS_GR), 0, L * 8, 0x00020000);
      gr0 = __builtin_amdgcn_raw_buffer_load_b128(rg, tid * 16, 0, 16);
      gr1 = __builtin_amdgcn_raw_buffer_load_b128(rg, tid * 16 + 8192, 0, 16);
    }
#pragma unroll
    for (int s = 0; s < 8; ++s)
      acc = __builtin_amdgcn_mfma_f32_32x32x16_f16(lds_ld<h16x8>(fa + 32 * s), lds_ld<h16x8>(fa + 21 * T_ROW + 32 * s), acc, 0, 0, 0);
    asm volatile("" ::: "memory");
  }
  ST(2);
  {
    const int c = lane & 31, h = lane >> 5;
    float k[12];
#pragma unroll
    for (int i = 0; i < 12; ++i) k[i] = acc[i] * unscale;
    if (lane == 0) k[0] += dsk;
    lds_st<u32x2>(krb + 2 * (316 - 8 * c - 4 * h), (u32x2){pk_bf(k[3], k[2]), pk_bf(k[1], k[0])});
    const u32 x0 = pk_bf(k[4], k[5]), x1 = pk_bf(k[6], k[7]), y0 = pk_bf(k[8], k[9]), y1 = pk_bf(k[10], k[11]);
    if (h == 0) {
      if (c > 0) {
        lds_st<u32>(ksb + 2 * (2 * c + 12), x0);
        lds_st<u32>(ksb + 192 + 2 * (2 * c + 12), x1);
      } else {
        lds_st<u32x4>(krb + 2 * 56, (u32x4){x1 & 0xffff0000u, (x0 >> 16) * 0x10001u, (x0 >> 16) | (x1 << 16), (x0 & 0xffffu) * 0x10001u});
        lds_st<u16>(ksb + 384 + 4 * 70 + 2, (u16)y0);
      }
      lds_st<u16>(ksb + 736 + 2 * (8 + c), (u16)y0);
      lds_st<u16>(ksb + 736 + 2 * (40 + c), (u16)(y0 >> 16));
      lds_st<u16>(ksb + 736 + 2 * (72 + c), (u16)y1);
      lds_st<u16>(ksb + 736 + 2 * (104 + c), (u16)(y1 >> 16));
    } else {
      if (c > 0) lds_st<u16>(ksb + 384 + 4 * (c + 6) + 2, (u16)x0);
      else { lds_st<u16>(ksb + 2 * 76, (u16)x0); lds_st<u16>(ksb + 192 + 2 * 76, (u16)x0); lds_st<u16>(ksb + 736 + 2 * 136, (u16)y0); }
      lds_st<u16>(ksb + 384 + 4 * (c + 7), (u16)(x0 >> 16));
      lds_st<u16>(ksb + 384 + 4 * (c + 38) + 2, (u16)x1);
      lds_st<u16>(ksb + 384 + 4 * (c + 39), (u16)(x1 >> 16));
    }
  }
  {
    const int y = 8 + 256 * w + lane;
    const u32 a = R_OFF + 8 * (y ^ (((y >> 5) & 1) << 2));
#pragma unroll
    for (int i = 0; i < 4; ++i) lds_st<u32x2>(a + 512 * i, ur[i]);
  }
  wg_barrier();
  ST(3);
  {
    const u64 T = lds_ld<u64>(MISC_OFF);
    const u32 thi = (u32)(T >> 32), tlo = (u32)T >> 16;
    auto rg = __builtin_amdgcn_make_buffer_rsrc((GAS void*)(ws + WS_GR), 0, L * 8, 0x00020000);
    for (int spin = 0; spin < 200000; ++spin) {
      bool ok = gr0.y == thi && gr0.w == thi && gr1.y == thi && gr1.w == thi &&
                (gr0.x >> 16) == tlo && (gr0.z >> 16) == tlo && (gr1.x >> 16) == tlo && (gr1.z >> 16) == tlo;
      if (__all(ok)) break;
      asm volatile("" ::: "memory");
      gr0 = __builtin_amdgcn_raw_buffer_load_b128(rg, tid * 16, 0, 16);
      gr1 = __builtin_amdgcn_raw_buffer_load_b128(rg, tid * 16 + 8192, 0, 16);
    }
    const int t = 2 * tid;
    const u32 a = G_OFF + 512 * (t >> 7) + 8 * (16 * ((t >> 4) & 3) + 8 * ((t >> 6) & 1) + (t & 7)) + 4 * ((t >> 3) & 1);
    lds_st<u32>(a, gr0.x << 16); lds_st<u32>(a + 8, gr0.z << 16);
    lds_st<u32>(a + 4096, gr1.x << 16); lds_st<u32>(a + 4096 + 8, gr1.z << 16);
  }
  ST(4);
  {
    u32 ra[4];
    const int G = lane >> 4, q = (lane >> 2) & 3, p = lane & 3;
#pragma unroll
    for (int n = 0; n < 4; ++n) { const int y = 32 * G + 8 * p + q + 4 * n; ra[n] = R_OFF + 8 * (y ^ (((y >> 5) & 1) << 2)); }
    const u32 wu = U_OFF + p * U_CH + 160 + 16 * (lane >> 2);
    const u32 wwp = (p << 16) | (160 + 16 * (lane >> 2));
    prep_block<true, 3>(w, ra, wu, wwp);
    if (w < 4) prep_block<true, 2>(w + 8, ra, wu, wwp);
    else if (w < 6) prep_block<true, 1>(w + 8, ra, wu, wwp);
    else prep_block<true, 0>(w + 8, ra, wu, wwp);
    if (tid < 280) {
      const int a = tid / 10, i = tid % 10;
      const u32 base = a < 16 ? U_OFF + (a >> 2) * U_CH + (a & 3) * U_R
                     : a < 20 ? W3_OFF + (a - 16) * W3_CH : a < 24 ? W5_OFF + (a - 20) * W5_CH : W7_OFF + (a - 24) * W7_CH;
      lds_st<u32x4>(base + 16 * i, (u32x4){0, 0, 0, 0});
    }
  }
  wg_barrier();
  ST(5);
  if (w < 4) {
    const int g = lane >> 4, abr = lane & 1, beta = (lane & 15) >> 1;
    const int ap = (lane >> 3) & 1, b = lane & 7, q = (lane >> 2) & 3, p = lane & 3;
    const u32 kr = KR_OFF + (2 * ch + abr) * KR_ST, ks = KS_OFF + (2 * ch + abr) * KS_ST;
    u32x4 A0[10], A1[5], A2[5], A3[8];
#pragma unroll
    for (int s = 0; s < 10; ++s) A0[s] = lds_ld<u32x4>(kr + 2 * (368 - 8 * (g + beta)) - 64 * s);
#pragma unroll
    for (int s = 0; s < 5; ++s) {
      const u32 a = ks + 2 * (4 * g + 2 * beta) + 32 * s;
      A1[s] = (u32x4){lds_ld<u32>(a), lds_ld<u32>(a + 4), lds_ld<u32>(a + 192), lds_ld<u32>(a + 196)};
    }
#pragma unroll
    for (int s = 0; s < 5; ++s) {
      const u32 a = ks + 384 + 4 * (4 * g + beta) + 64 * s;
      const u32 e0 = lds_ld<u32>(a), e1 = lds_ld<u32>(a + 4), e2 = lds_ld<u32>(a + 8), e3 = lds_ld<u32>(a + 12);
      A2[s] = (u32x4){__builtin_amdgcn_perm(e1, e0, 0x05040100), __builtin_amdgcn_perm(e3, e2, 0x05040100),
                      __builtin_amdgcn_perm(e1, e0, 0x07060302), __builtin_amdgcn_perm(e3, e2, 0x07060302)};
    }
    {
      const u32 k1024 = lds_ld<u16>(ks + 736 + 16);
      const int qq = beta - 4 * (g - 2);
      if (g >= 2 && qq >= 0 && qq < 4) { if (qq < 2) A2[4].x = k1024 << (16 * qq); else A2[4].y = k1024 << (16 * (qq - 2)); }
    }
    {
      const int m0 = -7 + 4 * g + beta, odd = m0 & 1;
      const u32 st = odd ? 0x05040504u : 0x05040100u, sc = odd ? 0x07060504u : 0x05040302u;
#pragma unroll
      for (int s = 0; s < 8; ++s) {
        const u32 a = ks + 736 + 2 * (m0 - odd + 8) + 32 * s;
        const u32 e0 = lds_ld<u32>(a), e1 = lds_ld<u32>(a + 4), e2 = lds_ld<u32>(a + 8);
        A3[s] = (u32x4){__builtin_amdgcn_perm(e1, e0, st), __builtin_amdgcn_perm(e2, e1, st),
                        __builtin_amdgcn_perm(e1, e0, sc), __builtin_amdgcn_perm(e2, e1, sc)};
      }
      const int qq = -1 - m0;
      const u32 k0 = qq == 0 ? 0xffff0000u : qq == 1 ? 0x0000ffffu : ~0u, k1 = qq == 2 ? 0xffff0000u : qq == 3 ? 0x0000ffffu : ~0u;
      A3[0].x &= k0; A3[0].z &= k0; A3[0].y &= k1; A3[0].w &= k1;
    }
    const u32 uc = U_OFF + ch * U_CH;
    const u32 b0 = uc + (3 - (b & 3)) * U_R + 2 * (64 * ap + 4 * (b >> 2) + 132 - 8 * g - 96);
    const int xb = 64 * (p >> 1) + 4 * (p & 1);
    const u32 b1w = W3_OFF + ch * W3_CH + 2 * (xb - 208 - 16 * g - 4 * q + 84 + 192);
    const u32 b1u = uc + 2 * U_R + 2 * (xb - 208 - 16 * g - 4 * q + 80 + 192);
    const u32 b2a = W3_OFF + ch * W3_CH + 2 * (xb - 460 - 32 * g - 8 * q + 84 + 512);
    const u32 b2b = W5_OFF + ch * W5_CH + 2 * (xb - 460 - 32 * g - 8 * q + 80 + 512);
    const u32 b2s = g < 2 ? b2a : uc + 3 * U_R + 2 * (xb + 8 * (q + 4 * (g - 2)) - 944 + 1024);
    const u32 b3u = uc + 2 * (xb + 52 - 32 * g - 8 * q + 80);
    const u32 b3w = W7_OFF + ch * W7_CH + 2 * (xb + 52 - 32 * g - 8 * q + 80);
    set_m0(56832 + 256 * ch);
    u32x4 B0[64], B1[28], B2[12], B3[8];
#pragma unroll
    for (int al = 0; al < 16; ++al) {
      f32x4 a0 = {0, 0, 0, 0}, a1 = {0, 0, 0, 0};
#pragma unroll
      for (int i = 0; i < 4; ++i) { const int x = 4 * al + i; B0[x] = cat(lds_ld<u32x2>(b0 + 64 * x), lds_ld<u32x2>(b0 + 64 * x + 8)); }
      if (al >= 2) {
#pragma unroll
        for (int i = 0; i < 2; ++i) { const int x = 2 * (al - 2) + i; B1[x] = cat(lds_tr(b1w + 128 * x), lds_tr(b1u + 128 * x)); }
      }
      if (al >= 4) { const int x = al - 4; B2[x] = cat(lds_tr(b2a + 256 * x), lds_tr(b2b + 256 * x)); }
      if (al >= 8) { const int x = al - 8; B3[x] = cat(lds_tr(b3u + 256 * x), lds_tr(b3w + 256 * x)); }
#pragma unroll
      for (int s = 0; s < 10; ++s) if (s < 4 * al + 4) a0 = mm(A0[s], B0[4 * al + 3 - s], a0);
#pragma unroll
      for (int s = 0; s < 5; ++s) if (s < 2 * al - 2) a1 = mm(A1[s], B1[2 * al - 3 - s], a1);
#pragma unroll
      for (int s = 0; s < 4; ++s) if (s < al - 3) a1 = mm(A2[s], B2[al - 4 - s], a1);
      if (al >= 8) { const int x = al - 8; a1 = mm(A2[4], cat(lds_tr(b2s + 256 * x), lds_tr(b2b + 256 * x)), a1); }
#pragma unroll
      for (int s = 0; s < 8; ++s) if (s < al - 7) a0 = mm(A3[s], B3[al - 8 - s], a0);
      a0 += a1;
      const f32x2 gg = lds_ld<f32x2>(G_OFF + 512 * al + 8 * lane);
      const u32 o = pk_bf(fmaf(gg[0], a0[0] - a0[1], a0[1]), fmaf(gg[1], a0[2] - a0[3], a0[3]));
      asm volatile("ds_write_addtid_b32 %0 offset:%1" ::"v"(o), "i"(49152 + 1024 * al) : "memory");
    }
  }
  ST(6);
  wg_barrier();
  {
    const int t1 = 64 * ((lane >> 3) & 1) + 16 * (lane >> 4) + (lane & 7);
#pragma unroll
    for (int i = 0; i < 2; ++i) {
      const int al = w + 8 * i;
      const u32 a = O_OFF + 1024 * al + 4 * lane;
      const u32 e0 = lds_ld<u32>(a), e1 = lds_ld<u32>(a + 256), e2 = lds_ld<u32>(a + 512), e3 = lds_ld<u32>(a + 768);
      GAS u16* po = out + (size_t)(128 * al + t1) * D + c0;
      *(GAS u32x2*)po = (u32x2){__builtin_amdgcn_perm(e1, e0, 0x05040100), __builtin_amdgcn_perm(e3, e2, 0x05040100)};
      *(GAS u32x2*)(po + 8 * D) = (u32x2){__builtin_amdgcn_perm(e1, e0, 0x07060302), __builtin_amdgcn_perm(e3, e2, 0x07060302)};
    }
  }
  asm volatile(".Lssm_code_b:");
  ST(7);
}

__global__ void nopk(char* p) { if (p == nullptr) __builtin_amdgcn_s_sleep(1); }
__global__ void __launch_bounds__(512) nopx(char* p) {  if (p == nullptr) __builtin_amdgcn_s_sleep(1); }
extern "C" void kernel_launch(void* const* d_in, const int* in_sizes, int n_in,
                              void* d_out, int out_size, void* d_ws, size_t ws_size,
                              hipStream_t stream) {
  const u16* const* in = (const u16* const*)d_in;
  (void)hipFuncSetAttribute((const void*)ssm_mix, hipFuncAttributeMaxDynamicSharedMemorySize, LDS_BYTES);
  ssm_mix<<<256, 512, LDS_BYTES, stream>>>(in[0], in[1], in[2], in[3], in[4], in[5], in[6], in[7], in[8],
                                           in[10], in[11], in[12], in[13], in[14], in[15], in[16],
                                           (u16*)d_out, (char*)d_ws);
  nopk<<<1, 64, 0, stream>>>((char*)d_ws);
  (void)hipFuncSetAttribute((const void*)nopx, hipFuncAttributeMaxDynamicSharedMemorySize, 163840);
  nopx<<<256, 512, 0, stream>>>((char*)d_ws);
}
```

```cpp
#include <hip/hip_runtime.h>
#include <hip/hip_bf16.h>
#include <stdint.h>
constexpr int L = 2048, D = 640, N = 192;
typedef unsigned long long u64;
__device__ __forceinline__ float bf2f(uint16_t v) { return __uint_as_float(((uint32_t)v) << 16); }
__device__ __forceinline__ float rbf(float x) { return __bfloat162float(__float2bfloat16(x)); }
__global__ void __launch_bounds__(256) slowk(const uint16_t* u, const uint16_t* Are, const uint16_t* Aim, const uint16_t* Bre, const uint16_t* Bim,
    const uint16_t* Cre, const uint16_t* Cim, const uint16_t* ldtA, const uint16_t* DA, const uint16_t* lAr, const uint16_t* Aimag,
    const uint16_t* Cr, const uint16_t* Ci, const uint16_t* ldtB, const uint16_t* DB, const uint16_t* gw, __hip_bfloat16* out) {
  u64 t0; asm volatile("s_memrealtime %0\n\ts_waitcnt lgkmcnt(0)" : "=s"(t0) :: "memory");

  extern __shared__ float sm[];
  float* su = sm; float* kk = sm + L; float* g = sm + 3 * L; float* pr = sm + 4 * L;
  const int t = threadIdx.x;
  for (int l = t; l < L; l += 256) { float a = 0.f; for (int d = 0; d < D; d += 2) { uint32_t x = *(const uint32_t*)(u + l * D + d), w = *(const uint32_t*)(gw + d); a += bf2f(x) * bf2f(w) + bf2f(x >> 16) * bf2f(w >> 16); } g[l] = rbf(1.f / (1.f + __expf(-rbf(a)))); }
  for (int d = blockIdx.x; d < D; d += gridDim.x) {
    __syncthreads();
    for (int i = t; i < 2 * N; i += 256) { int br = i / N, j = d * N + i % N; float* q = pr + 4 * i;
      if (br == 0) { float dt = rbf(expf(bf2f(ldtA[d]))); q[0] = dt * bf2f(Are[j]); q[1] = dt * bf2f(Aim[j]); float b0 = dt * bf2f(Bre[j]), b1 = dt * bf2f(Bim[j]), c0 = bf2f(Cre[j]), c1 = bf2f(Cim[j]); q[2] = c0 * b0 - c1 * b1; q[3] = c0 * b1 + c1 * b0; }
      else { float dt = rbf(expf(bf2f(ldtB[d]))); q[0] = -rbf(expf(bf2f(lAr[j]))) * dt; q[1] = dt * bf2f(Aimag[j]); q[2] = 2.f * dt * bf2f(Cr[j]); q[3] = 2.f * dt * bf2f(Ci[j]); } }
    for (int l = t; l < L; l += 256) su[l] = bf2f(u[l * D + d]);
    __syncthreads();
    for (int i = t; i < 2 * L; i += 256) { int br = i / L; float p = rbf((float)(i % L)), a = 0.f; for (int n = 0; n < N; ++n) { const float* q = pr + 4 * (br * N + n); float e = __expf(q[0] * p); a += e * (q[2] * __cosf(q[1] * p) - q[3] * __sinf(q[1] * p)); } kk[i] = a; }
    __syncthreads();
    float da = bf2f(DA[d]), db = bf2f(DB[d]);
    for (int l = t; l < L; l += 256) { float ya = 0.f, yb = 0.f; for (int j = 0; j <= l; ++j) { float x = su[l - j]; ya += kk[j] * x; yb += kk[L + j] * x; }
      out[l * D + d] = __float2bfloat16(g[l] * (ya + su[l] * da) + (1.f - g[l]) * (yb + su[l] * db)); }
  }
  asm volatile("s_waitcnt vmcnt(0)" ::: "memory"); __syncthreads(); if (t == 0) { __builtin_amdgcn_fence(__ATOMIC_RELEASE, "agent"); asm volatile("s_waitcnt vmcnt(0)" ::: "memory"); }
  while (__builtin_amdgcn_s_memrealtime() - t0 < 200000) __builtin_amdgcn_s_sleep(1);
}
extern "C" void kernel_launch(void* const* d_in, const int* in_sizes, int n_in, void* d_out, int out_size, void* d_ws, size_t ws_size, hipStream_t stream) {
  const uint16_t* const* in = (const uint16_t* const*)d_in;
  (void)hipFuncSetAttribute((const void*)slowk, hipFuncAttributeMaxDynamicSharedMemorySize, 163840);
  slowk<<<256, 256, 163840, stream>>>(in[0], in[1], in[2], in[3], in[4], in[5], in[6], in[7], in[8], in[10], in[11], in[12], in[13], in[14], in[15], in[16], (__hip_bfloat16*)d_out);
}
```

```cpp
#include <hip/hip_runtime.h>
#include <hip/hip_bf16.h>
#include <stdint.h>
#pragma clang diagnostic ignored "-Wint-to-pointer-cast"
#pragma clang diagnostic ignored "-Winline-asm"

typedef unsigned long long u64;
typedef unsigned u32;
typedef unsigned short u16;
typedef u32 u32x4 __attribute__((ext_vector_type(4)));
typedef float f32x2 __attribute__((ext_vector_type(2)));
typedef __bf16 b16x2 __attribute__((ext_vector_type(2)));
#define GAS __attribute__((address_space(1)))
#define CAS __attribute__((address_space(4)))
#define RLX_AGENT __ATOMIC_RELAXED, __HIP_MEMORY_SCOPE_AGENT
#define DEV __device__ __forceinline__

constexpr int L = 2048, D = 640;
constexpr int LDS_BYTES = 163840;
constexpr int WS_ACK = 4096, WS_GR = 8192, WS_STAMP = 4 << 20;
constexpr int J0 = 12;

DEV u32 pk_bf(float a, float b) { f32x2 x = {a, b}; b16x2 y = __builtin_convertvector(x, b16x2); return __builtin_bit_cast(u32, y); }

static __device__ const u32 lane_tab[1280] __attribute__((aligned(256))) = {
  0x40, 0x80, 0xf0, 0x23750, 0xc0, 0x88, 0xf8, 0x23760,
  0x50, 0x100, 0x170, 0x23770, 0xd0, 0x108, 0x178, 0x23780,
  0x60, 0x78, 0xe0, 0x23790, 0xe0, 0x80, 0xe8, 0x237a0,
  0x70, 0xf8, 0x160, 0x237b0, 0xf0, 0x100, 0x168, 0x237c0,
  0x80, 0x70, 0xd0, 0x237d0, 0x100, 0x78, 0xd8, 0x237e0,
  0x90, 0xf0, 0x150, 0x237f0, 0x110, 0xf8, 0x158, 0x23800,
  0xa0, 0x68, 0xc0, 0x23810, 0x120, 0x70, 0xc8, 0x23820,
  0xb0, 0xe8, 0x140, 0x23830, 0x130, 0xf0, 0x148, 0x23840,
  0x30, 0x60, 0xb0, 0x23850, 0xb0, 0x68, 0xb8, 0x23860,
  0x40, 0xe0, 0x130, 0x23870, 0xc0, 0xe8, 0x138, 0x23880,
  0x50, 0x58, 0xa0, 0x23890, 0xd0, 0x60, 0xa8, 0x238a0,
  0x60, 0xd8, 0x120, 0x238b0, 0xe0, 0xe0, 0x128, 0x238c0,
  0x70, 0x50, 0x90, 0x238d0, 0xf0, 0x58, 0x98, 0x238e0,
  0x80, 0xd0, 0x110, 0x238f0, 0x100, 0xd8, 0x118, 0x23900,
  0x90, 0x48, 0x80, 0x23910, 0x110, 0x50, 0x88, 0x23920,
  0xa0, 0xc8, 0x100, 0x23930, 0x120, 0xd0, 0x108, 0x23940,
  0x20, 0x40, 0x70, 0x23950, 0xa0, 0x48, 0x78, 0x23960,
  0x30, 0xc0, 0xf0, 0x23970, 0xb0, 0xc8, 0xf8, 0x23980,
  0x40, 0x38, 0x60, 0x23990, 0xc0, 0x40, 0x68, 0x239a0,
  0x50, 0xb8, 0xe0, 0x239b0, 0xd0, 0xc0, 0xe8, 0x239c0,
  0x60, 0x30, 0x50, 0x239d0, 0xe0, 0x38, 0x58, 0x239e0,
  0x70, 0xb0, 0xd0, 0x239f0, 0xf0, 0xb8, 0xd8, 0x23a00,
  0x80, 0x28, 0x40, 0x23a10, 0x100, 0x30, 0x48, 0x23a20,
  0x90, 0xa8, 0xc0, 0x23a30, 0x110, 0xb0, 0xc8, 0x23a40,
  0x10, 0x20, 0x30, 0x23a50, 0x90, 0x28, 0x38, 0x23a60,
  0x20, 0xa0, 0xb0, 0x23a70, 0xa0, 0xa8, 0xb8, 0x23a80,
  0x30, 0x18, 0x20, 0x23a90, 0xb0, 0x20, 0x28, 0x23aa0,
  0x40, 0x98, 0xa0, 0x23ab0, 0xc0, 0xa0, 0xa8, 0x23ac0,
  0x50, 0x10, 0x10, 0x23ad0, 0xd0, 0x18, 0x18, 0x23ae0,
  0x60, 0x90, 0x90, 0x23af0, 0xe0, 0x98, 0x98, 0x23b00,
  0x70, 0x8, 0x0, 0x23b10, 0xf0, 0x10, 0x8, 0x23b20,
  0x80, 0x88, 0x80, 0x23b30, 0x100, 0x90, 0x88, 0x23b40,
  0x1adec, 0x1d150, 0x1d2d0, 0x1d430, 0x1ab8c, 0x1d590, 0x1d710, 0x1d870,
  0x1ade8, 0x1d154, 0x1d2d4, 0x1d434, 0x1ab88, 0x1d594, 0x1d714, 0x1d874,
  0x1ade4, 0x1d158, 0x1d2d8, 0x1d434, 0x1ab84, 0x1d598, 0x1d718, 0x1d874,
  0x1ade0, 0x1d15c, 0x1d2dc, 0x1d438, 0x1ab80, 0x1d59c, 0x1d71c, 0x1d878,
  0x1b2ac, 0x1d160, 0x1d2e0, 0x1d438, 0x1b04c, 0x1d5a0, 0x1d720, 0x1d878,
  0x1b2a8, 0x1d164, 0x1d2e4, 0x1d43c, 0x1b048, 0x1d5a4, 0x1d724, 0x1d87c,
  0x1b2a4, 0x1d168, 0x1d2e8, 0x1d43c, 0x1b044, 0x1d5a8, 0x1d728, 0x1d87c,
  0x1b2a0, 0x1d16c, 0x1d2ec, 0x1d440, 0x1b040, 0x1d5ac, 0x1d72c, 0x1d880,
  0x1addc, 0x1d158, 0x1d2e0, 0x1d438, 0x1ab7c, 0x1d598, 0x1d720, 0x1d878,
  0x1add8, 0x1d15c, 0x1d2e4, 0x1d43c, 0x1ab78, 0x1d59c, 0x1d724, 0x1d87c,
  0x1add4, 0x1d160, 0x1d2e8, 0x1d43c, 0x1ab74, 0x1d5a0, 0x1d728, 0x1d87c,
  0x1add0, 0x1d164, 0x1d2ec, 0x1d440, 0x1ab70, 0x1d5a4, 0x1d72c, 0x1d880,
  0x1b29c, 0x1d168, 0x1d2f0, 0x1d440, 0x1b03c, 0x1d5a8, 0x1d730, 0x1d880,
  0x1b298, 0x1d16c, 0x1d2f4, 0x1d444, 0x1b038, 0x1d5ac, 0x1d734, 0x1d884,
  0x1b294, 0x1d170, 0x1d2f8, 0x1d444, 0x1b034, 0x1d5b0, 0x1d738, 0x1d884,
  0x1b290, 0x1d174, 0x1d2fc, 0x1d448, 0x1b030, 0x1d5b4, 0x1d73c, 0x1d888,
  0x1adcc, 0x1d160, 0x1d2f0, 0x1d440, 0x1ab6c, 0x1d5a0, 0x1d730, 0x1d880,
  0x1adc8, 0x1d164, 0x1d2f4, 0x1d444, 0x1ab68, 0x1d5a4, 0x1d734, 0x1d884,
  0x1adc4, 0x1d168, 0x1d2f8, 0x1d444, 0x1ab64, 0x1d5a8, 0x1d738, 0x1d884,
  0x1adc0, 0x1d16c, 0x1d2fc, 0x1d448, 0x1ab60, 0x1d5ac, 0x1d73c, 0x1d888,
  0x1b28c, 0x1d170, 0x1d300, 0x1d448, 0x1b02c, 0x1d5b0, 0x1d740, 0x1d888,
  0x1b288, 0x1d174, 0x1d304, 0x1d44c, 0x1b028, 0x1d5b4, 0x1d744, 0x1d88c,
  0x1b284, 0x1d178, 0x1d308, 0x1d44c, 0x1b024, 0x1d5b8, 0x1d748, 0x1d88c,
  0x1b280, 0x1d17c, 0x1d30c, 0x1d450, 0x1b020, 0x1d5bc, 0x1d74c, 0x1d890,
  0x1adbc, 0x1d168, 0x1d300, 0x1d448, 0x1ab5c, 0x1d5a8, 0x1d740, 0x1d888,
  0x1adb8, 0x1d16c, 0x1d304, 0x1d44c, 0x1ab58, 0x1d5ac, 0x1d744, 0x1d88c,
  0x1adb4, 0x1d170, 0x1d308, 0x1d44c, 0x1ab54, 0x1d5b0, 0x1d748, 0x1d88c,
  0x1adb0, 0x1d174, 0x1d30c, 0x1d450, 0x1ab50, 0x1d5b4, 0x1d74c, 0x1d890,
  0x1b27c, 0x1d178, 0x1d310, 0x1d450, 0x1b01c, 0x1d5b8, 0x1d750, 0x1d890,
  0x1b278, 0x1d17c, 0x1d314, 0x1d454, 0x1b018, 0x1d5bc, 0x1d754, 0x1d894,
  0x1b274, 0x1d180, 0x1d318, 0x1d454, 0x1b014, 0x1d5c0, 0x1d758, 0x1d894,
  0x1b270, 0x1d184, 0x1d31c, 0x1d458, 0x1b010, 0x1d5c4, 0x1d75c, 0x1d898,
  0x1d150, 0x5040504, 0x7060504, 0x5040504, 0x1d150, 0x5040504, 0x7060504, 0x5040504,
  0x1d150, 0x5040100, 0x5040302, 0x5040100, 0x1d150, 0x5040100, 0x5040302, 0x5040100,
  0x1d150, 0x5040504, 0x7060504, 0x5040504, 0x1d150, 0x5040504, 0x7060504, 0x5040504,
  0x1d150, 0x5040100, 0x5040302, 0x5040100, 0x1d150, 0x5040100, 0x5040302, 0x5040100,
  0x1d590, 0x5040504, 0x7060504, 0x5040504, 0x1d590, 0x5040504, 0x7060504, 0x5040504,
  0x1d590, 0x5040100, 0x5040302, 0xc0c0100, 0x1d590, 0x5040100, 0x5040302, 0xc0c0100,
  0x1d590, 0x5040504, 0x7060504, 0x5040c0c, 0x1d590, 0x5040504, 0x7060504, 0x5040c0c,
  0x1d590, 0x5040100, 0x5040302, 0x5040100, 0x1d590, 0x5040100, 0x5040302, 0x5040100,
  0x1d150, 0x5040504, 0x7060504, 0x5040504, 0x1d150, 0x5040504, 0x7060504, 0x5040504,
  0x1d150, 0x5040100, 0x5040302, 0xc0c0100, 0x1d150, 0x5040100, 0x5040302, 0xc0c0100,
  0x1d150, 0x5040504, 0x7060504, 0x5040c0c, 0x1d150, 0x5040504, 0x7060504, 0x5040c0c,
  0x1d150, 0x5040100, 0x5040302, 0x5040100, 0x1d150, 0x5040100, 0x5040302, 0x5040100,
  0x1d590, 0x5040504, 0x7060504, 0x5040504, 0x1d590, 0x5040504, 0x7060504, 0x5040504,
  0x1d590, 0x5040100, 0x5040302, 0x5040100, 0x1d590, 0x5040100, 0x5040302, 0x5040100,
  0x1d590, 0x5040504, 0x7060504, 0x5040504, 0x1d590, 0x5040504, 0x7060504, 0x5040504,
  0x1d590, 0x5040100, 0x5040302, 0x5040100, 0x1d590, 0x5040100, 0x5040302, 0x5040100,
  0x1d150, 0x5040504, 0x7060504, 0x5040504, 0x1d150, 0x5040504, 0x7060504, 0x5040504,
  0x1d150, 0x5040100, 0x5040302, 0x5040100, 0x1d150, 0x5040100, 0x5040302, 0x5040100,
  0x1d150, 0x5040504, 0x7060504, 0x5040504, 0x1d150, 0x5040504, 0x7060504, 0x5040504,
  0x1d150, 0x5040100, 0x5040302, 0x5040100, 0x1d150, 0x5040100, 0x5040302, 0x5040100,
  0x1d590, 0x5040504, 0x7060504, 0x5040504, 0x1d590, 0x5040504, 0x7060504, 0x5040504,
  0x1d590, 0x5040100, 0x5040302, 0x5040100, 0x1d590, 0x5040100, 0x5040302, 0x5040100,
  0x1d590, 0x5040504, 0x7060504, 0x5040504, 0x1d590, 0x5040504, 0x7060504, 0x5040504,
  0x1d590, 0x5040100, 0x5040302, 0x5040100, 0x1d590, 0x5040100, 0x5040302, 0x5040100,
  0x1d150, 0x5040504, 0x7060504, 0x5040504, 0x1d150, 0x5040504, 0x7060504, 0x5040504,
  0x1d150, 0x5040100, 0x5040302, 0x5040100, 0x1d150, 0x5040100, 0x5040302, 0x5040100,
  0x1d150, 0x5040504, 0x7060504, 0x5040504, 0x1d150, 0x5040504, 0x7060504, 0x5040504,
  0x1d150, 0x5040100, 0x5040302, 0x5040100, 0x1d150, 0x5040100, 0x5040302, 0x5040100,
  0x1d590, 0x5040504, 0x7060504, 0x5040504, 0x1d590, 0x5040504, 0x7060504, 0x5040504,
  0x1d590, 0x5040100, 0x5040302, 0x5040100, 0x1d590, 0x5040100, 0x5040302, 0x5040100,
  0x1d590, 0x5040504, 0x7060504, 0x5040504, 0x1d590, 0x5040504, 0x7060504, 0x5040504,
  0x1d590, 0x5040100, 0x5040302, 0x5040100, 0x1d590, 0x5040100, 0x5040302, 0x5040100,
  0x5040504, 0x7060504, 0x7060504, 0x0, 0x5040504, 0x7060504, 0x7060504, 0x0,
  0x5040100, 0x5040302, 0x5040302, 0x0, 0x5040100, 0x5040302, 0x5040302, 0xffff,
  0x5040504, 0x7060504, 0x7060504, 0xffff0000, 0x5040504, 0x7060504, 0x7060504, 0x0,
  0xc0c0100, 0x5040302, 0xc0c0302, 0x0, 0xc0c0100, 0x5040302, 0xc0c0302, 0x0,
  0x5040c0c, 0x7060504, 0x7060c0c, 0x0, 0x5040c0c, 0x7060504, 0x7060c0c, 0x0,
  0x5040100, 0xc0c0302, 0x5040302, 0x0, 0x5040100, 0xc0c0302, 0x5040302, 0xffff,
  0x5040504, 0x7060c0c, 0x7060504, 0xffff0000, 0x5040504, 0x7060c0c, 0x7060504, 0x0,
  0x5040100, 0x5040302, 0x5040302, 0x0, 0x5040100, 0x5040302, 0x5040302, 0x0,
  0x5040c0c, 0x7060504, 0x7060c0c, 0x0, 0x5040c0c, 0x7060504, 0x7060c0c, 0x0,
  0x5040100, 0xc0c0302, 0x5040302, 0x0, 0x5040100, 0xc0c0302, 0x5040302, 0x0,
  0x5040504, 0x7060c0c, 0x7060504, 0x0, 0x5040504, 0x7060c0c, 0x7060504, 0x0,
  0x5040100, 0x5040302, 0x5040302, 0x0, 0x5040100, 0x5040302, 0x5040302, 0x0,
  0x5040504, 0x7060504, 0x7060504, 0x0, 0x5040504, 0x7060504, 0x7060504, 0x0,
  0x5040100, 0x5040302, 0x5040302, 0x0, 0x5040100, 0x5040302, 0x5040302, 0x0,
  0x5040504, 0x7060504, 0x7060504, 0x0, 0x5040504, 0x7060504, 0x7060504, 0x0,
  0x5040100, 0x5040302, 0x5040302, 0x0, 0x5040100, 0x5040302, 0x5040302, 0x0,
  0x5040504, 0x7060504, 0x7060504, 0x0, 0x5040504, 0x7060504, 0x7060504, 0x0,
  0x5040100, 0x5040302, 0x5040302, 0x0, 0x5040100, 0x5040302, 0x5040302, 0x0,
  0x5040504, 0x7060504, 0x7060504, 0x0, 0x5040504, 0x7060504, 0x7060504, 0x0,
  0x5040100, 0x5040302, 0x5040302, 0x0, 0x5040100, 0x5040302, 0x5040302, 0x0,
  0x5040504, 0x7060504, 0x7060504, 0x0, 0x5040504, 0x7060504, 0x7060504, 0x0,
  0x5040100, 0x5040302, 0x5040302, 0x0, 0x5040100, 0x5040302, 0x5040302, 0x0,
  0x5040504, 0x7060504, 0x7060504, 0x0, 0x5040504, 0x7060504, 0x7060504, 0x0,
  0x5040100, 0x5040302, 0x5040302, 0x0, 0x5040100, 0x5040302, 0x5040302, 0x0,
  0x5040504, 0x7060504, 0x7060504, 0x0, 0x5040504, 0x7060504, 0x7060504, 0x0,
  0x5040100, 0x5040302, 0x5040302, 0x0, 0x5040100, 0x5040302, 0x5040302, 0x0,
  0x5040504, 0x7060504, 0x7060504, 0x0, 0x5040504, 0x7060504, 0x7060504, 0x0,
  0x5040100, 0x5040302, 0x5040302, 0x0, 0x5040100, 0x5040302, 0x5040302, 0x0,
  0x5040504, 0x7060504, 0x7060504, 0x0, 0x5040504, 0x7060504, 0x7060504, 0x0,
  0x5040100, 0x5040302, 0x5040302, 0x0, 0x5040100, 0x5040302, 0x5040302, 0x0,
  0x5040504, 0x7060504, 0x7060504, 0x0, 0x5040504, 0x7060504, 0x7060504, 0x0,
  0x5040100, 0x5040302, 0x5040302, 0x0, 0x5040100, 0x5040302, 0x5040302, 0x0,
  0x0, 0x0, 0x0, 0x0, 0x0, 0x0, 0x0, 0x0,
  0x0, 0x0, 0x0, 0x0, 0x0, 0x0, 0x0, 0x0,
  0x0, 0x0, 0x0, 0x0, 0xffff, 0x0, 0x0, 0x0,
  0xffff0000, 0x0, 0x0, 0x0, 0x0, 0xffff, 0x0, 0x0,
  0x0, 0x0, 0x0, 0x0, 0x0, 0x0, 0x0, 0x0,
  0x0, 0x0, 0x0, 0x0, 0x0, 0x0, 0x0, 0x0,
  0x0, 0x0, 0x0, 0x0, 0xffff, 0x0, 0x0, 0x0,
  0xffff0000, 0x0, 0x0, 0x0, 0x0, 0xffff, 0x0, 0x0,
  0x0, 0xffff0000, 0x0, 0x0, 0x0, 0x0, 0xffff, 0x0,
  0x0, 0x0, 0xffff0000, 0x0, 0x0, 0x0, 0x0, 0x0,
  0x0, 0x0, 0x0, 0x0, 0x0, 0x0, 0x0, 0x0,
  0x0, 0x0, 0x0, 0x0, 0x0, 0x0, 0x0, 0x0,
  0x0, 0xffff0000, 0x0, 0x0, 0x0, 0x0, 0xffff, 0x0,
  0x0, 0x0, 0xffff0000, 0x0, 0x0, 0x0, 0x0, 0x0,
  0x0, 0x0, 0x0, 0x0, 0x0, 0x0, 0x0, 0x0,
  0x0, 0x0, 0x0, 0x0, 0x0, 0x0, 0x0, 0x0,
  0x0, 0x0, 0x0, 0x0, 0x0, 0x0, 0x0, 0x0,
  0x0, 0x0, 0x0, 0x0, 0x0, 0x0, 0x0, 0x0,
  0x0, 0x0, 0x0, 0x0, 0x0, 0x0, 0x0, 0x0,
  0x0, 0x0, 0x0, 0x0, 0x0, 0x0, 0x0, 0x0,
  0x0, 0x0, 0x0, 0x0, 0x0, 0x0, 0x0, 0x0,
  0x0, 0x0, 0x0, 0x0, 0x0, 0x0, 0x0, 0x0,
  0x0, 0x0, 0x0, 0x0, 0x0, 0x0, 0x0, 0x0,
  0x0, 0x0, 0x0, 0x0, 0x0, 0x0, 0x0, 0x0,
  0x0, 0x0, 0x0, 0x0, 0x0, 0x0, 0x0, 0x0,
  0x0, 0x0, 0x0, 0x0, 0x0, 0x0, 0x0, 0x0,
  0x0, 0x0, 0x0, 0x0, 0x0, 0x0, 0x0, 0x0,
  0x0, 0x0, 0x0, 0x0, 0x0, 0x0, 0x0, 0x0,
  0x0, 0x0, 0x0, 0x0, 0x0, 0x0, 0x0, 0x0,
  0x0, 0x0, 0x0, 0x0, 0x0, 0x0, 0x0, 0x0,
  0x0, 0x0, 0x0, 0x0, 0x0, 0x0, 0x0, 0x0,
  0x0, 0x0, 0x0, 0x0, 0x0, 0x0, 0x0, 0x0};

__global__ void __launch_bounds__(512, 2)
ssm_mix(const u16* a0, const u16* a1, const u16* a2, const u16* a3, const u16* a4, const u16* a5, const u16* a6,
        const u16* a7, const u16* b0, const u16* b1, const u16* b2, const u16* b3, const u16* b4, const u16* b5,
        const u16* b6, const u16* b7, const u16* u_, u16* out_, char* ws_, const u16* gw_) {
  const int tid = threadIdx.x;
  asm volatile(R"ASM(
.Lk_begin:
	s_add_i32 s37, %1, -8*12
	s_cmp_lt_u32 s37, 160
	s_mov_b64 s[62:63], %0
	s_mov_b32 s37, %1
	s_cbranch_scc0 .Lk_help
	s_mov_b64 s[4:5], %3
	v_readfirstlane_b32 s32, v0
	s_load_dwordx8 s[24:31], s[62:63], 0x80
	s_lshr_b32 s32, s32, 6
	s_lshr_b32 s34, s32, 2
	s_lshl_b32 s42, s34, 6
	s_add_u32 s62, s62, s42
	s_addc_u32 s63, s63, 0
	s_load_dwordx16 s[8:23], s[62:63], 0x0
	s_and_b32 s33, s32, 3
	s_and_b32 s42, s37, 7
	s_mul_i32 s42, s42, 80
	s_lshr_b32 s43, s37, 3
	s_lshl2_add_u32 s35, s43, s42
	s_add_i32 s35, s35, -48
	s_add_i32 s36, s35, s33
	s_mul_i32 s42, s36, 384
	v_and_b32 v1, 63, v0
	v_lshl_add_u32 v56, v1, 2, s42
	v_lshl_add_u32 v57, v1, 1, s42
	v_mov_b64 v[154:155], 0
	v_mov_b64 v[160:161], 0
	v_mov_b64 v[166:167], 0
	s_getpc_b64 s[44:45]
.Lk_pc:
	s_lshr_b32 s43, s37, 3
	s_add_i32 s43, s43, -12
	s_lshl3_add_u32 s43, s43, s32
	s_lshl_b32 s43, s43, 7
	s_min_u32 s43, s43, .Lk_end-.Lk_pc-4
	v_mov_b32 v255, s43
	s_mov_b64 exec, 1
	global_load_dword v255, v255, s[44:45]
	s_mov_b64 exec, -1
	s_lshl_b32 s43, s36, 1
	v_mov_b32 v58, s43
	v_mov_b32 v59, 0
	v_mov_b32 v251, 0
	v_mov_b32 v60, 0
	s_cmp_eq_u32 s34, 0
	s_waitcnt lgkmcnt(0)
	global_load_short_d16_hi v59, v58, s[16:17]
	global_load_dword v144, v56, s[8:9]
	global_load_dword v145, v56, s[10:11]
	global_load_dword v146, v56, s[12:13]
	global_load_dword v147, v56, s[14:15]
	s_cbranch_scc0 .Lk_nob
	global_load_dword v148, v56, s[20:21]
	global_load_dword v149, v56, s[22:23]
	global_load_short_d16_hi v154, v57, s[8:9] offset:256
	global_load_short_d16_hi v155, v57, s[10:11] offset:256
	global_load_short_d16_hi v160, v57, s[12:13] offset:256
	global_load_short_d16_hi v161, v57, s[14:15] offset:256
	global_load_short_d16_hi v166, v57, s[20:21] offset:256
	global_load_short_d16_hi v167, v57, s[22:23] offset:256
.Lk_nob:
	s_mov_b64 exec, 1
	global_load_short_d16_hi v251, v58, s[18:19]
	s_mov_b64 exec, -1
	global_load_dwordx2 v[52:53], v60, s[28:29]
	s_lshl1_add_u32 s42, s33, s34
	s_mul_i32 s51, s42, 1216
	s_add_i32 s51, s51, 109392
	s_mul_i32 s52, s42, 1088
	s_add_i32 s52, s52, 119120
	s_mul_i32 s53, s33, 17312
	s_lshl_b32 s49, s32, 8
	s_add_i32 s50, s49, 45408
	s_mov_b32 s58, 0x05040100
	s_mov_b32 s59, 0x07060302
	s_mov_b32 s60, 0xffff0000
	s_add_u32 s56, s28, 8192
	s_addc_u32 s57, s29, 0
	s_mov_b32 s55, s34
	v_mov_b64 v[240:241], 0
	v_mov_b64 v[242:243], 0
	v_lshl_add_u32 v62, v1, 4, s52
	v_lshl_add_u32 v63, v1, 4, s51
	ds_write_b128 v62, v[240:243]
	ds_write_b128 v63, v[240:243]
	s_mov_b64 exec, 0xfff
	ds_write_b128 v63, v[240:243] offset:1024
	s_mov_b64 exec, 15
	ds_write_b128 v62, v[240:243] offset:1024
	s_mov_b64 exec, -1
	s_mov_b32 m0, s49
	v_mov_b32 v62, 0x3c00
	ds_write_addtid_b32 v62 offset:43344
	v_and_b32 v62, 31, v1
	v_lshrrev_b32 v63, 5, v1
	v_mul_u32_u24 v62, 2064, v62
	v_lshl_add_u32 v62, v63, 4, v62
	v_add_u32 v30, s49, v62
	v_and_b32 v236, 31, v1
	v_lshrrev_b32 v237, 5, v1
	s_add_i32 s42, s51, 568
	v_lshlrev_b32 v62, 4, v236
	v_lshl_add_u32 v62, v237, 3, v62
	v_sub_u32 v244, s42, v62
	v_lshl_add_u32 v62, v236, 2, s52
	v_mul_u32_u24 v63, 388, v237
	v_add3_u32 v245, v62, v63, 24
	v_mul_u32_u24 v63, 324, v237
	v_add_u32 v63, 216, v63
	v_add_u32 v246, v62, v63
	s_add_i32 s42, s52, 152
	v_writelane_b32 v245, s42, 0
	s_add_i32 s43, s52, 344
	v_writelane_b32 v246, s43, 0
	v_and_b32 v62, 30, v1
	v_lshlrev_b32 v62, 1, v62
	v_and_b32 v63, 1, v1
	v_lshl_add_u32 v62, v63, 6, v62
	v_add_u32 v247, s52, v62
	s_add_i32 s42, s52, 256
	v_writelane_b32 v247, s42, 32
	s_add_i32 s43, s52, -84
	v_writelane_b32 v247, s43, 33
	v_cmp_eq_u32 vcc, 1, v63
	v_mov_b32 v62, 0x03020706
	v_mov_b32 v63, s58
	v_mov_b32 v250, -1
	v_cndmask_b32 v254, v63, v62, vcc
	s_mov_b32 s42, 0x0c0c0100
	v_writelane_b32 v250, s60, 32
	v_writelane_b32 v254, s42, 32
	v_writelane_b32 v254, s42, 33
	v_mov_b32 v249, 0x03020302
	v_mov_b32 v253, 0x03020302
	s_mov_b32 s42, 0x07060c0c
	v_writelane_b32 v249, s42, 0
	s_mov_b32 s42, 0x03020706
	v_writelane_b32 v253, s42, 0
	s_mov_b32 s43, 0x05040302
	v_writelane_b32 v249, s43, 2
	s_mov_b32 s43, 0x01000504
	v_writelane_b32 v253, s43, 2
	s_mov_b32 s42, 0x01000100
	v_writelane_b32 v249, s42, 3
	v_writelane_b32 v253, s42, 3
	s_cmp_eq_u32 s34, 0
	s_cbranch_scc0 .Lk_sb
	s_setprio 3
	v_mov_b32 v62, 0x3fb8aa3b
	s_waitcnt vmcnt(2)
	v_mul_f32 v63, v59, v62
	v_exp_f32 v63, v63
	v_lshlrev_b32 v150, 16, v144
	v_and_b32 v152, s60, v144
	v_lshlrev_b32 v151, 16, v145
	v_and_b32 v153, s60, v145
	v_lshlrev_b32 v156, 16, v146
	v_and_b32 v158, s60, v146
	v_lshlrev_b32 v157, 16, v147
	v_and_b32 v159, s60, v147
	v_cvt_pk_bf16_f32 v63, v63, v63
	v_lshlrev_b32 v63, 16, v63
	v_mul_f32 v188, v62, v63
	v_mul_f32 v189, 0x3e22f983, v63
	v_readfirstlane_b32 s40, v63
	v_lshlrev_b32 v162, 16, v148
	v_and_b32 v164, s60, v148
	v_lshlrev_b32 v163, 16, v149
	v_and_b32 v165, s60, v149
	v_pk_mul_f32 v[150:151], v[150:151], v[188:189]
	v_pk_mul_f32 v[152:153], v[152:153], v[188:189]
	v_pk_mul_f32 v[154:155], v[154:155], v[188:189]
	v_pk_mul_f32 v[144:145], v[156:157], v[162:163] op_sel_hi:[1,0]
	v_pk_fma_f32 v[156:157], v[156:157], v[162:163], v[144:145] op_sel:[1,1,0] op_sel_hi:[0,1,1] neg_lo:[1,0,0]
	v_pk_mul_f32 v[146:147], v[158:159], v[164:165] op_sel_hi:[1,0]
	v_pk_fma_f32 v[158:159], v[158:159], v[164:165], v[146:147] op_sel:[1,1,0] op_sel_hi:[0,1,1] neg_lo:[1,0,0]
	v_pk_mul_f32 v[148:149], v[160:161], v[166:167] op_sel_hi:[1,0]
	v_pk_fma_f32 v[160:161], v[160:161], v[166:167], v[148:149] op_sel:[1,1,0] op_sel_hi:[0,1,1] neg_lo:[1,0,0]
	v_max3_f32 v63, v150, v152, v154
	v_max_f32 v63, 0, v63
	v_mov_b32 v62, 0x3f7fbe77
	v_fmac_f32 v62, 0x45000000, v63
	v_min_f32 v62, 0x43800000, v62
	v_cvt_i32_f32 v62, v62
	v_max3_f32 v63, |v156|, |v157|, |v158|
	v_max3_f32 v63, v63, |v159|, |v160|
	v_max3_f32 v63, v63, |v161|, |v161|
	v_frexp_exp_i32_f32 v63, v63
	v_add_u32 v63, v63, v62
	v_fract_f32 v151, v151
	v_exp_f32 v150, v150
	v_max_i32_dpp v63, v63, v63 row_shr:1 row_mask:0xf bank_mask:0xf
	v_cos_f32 v182, v151
	v_sin_f32 v183, v151
	v_max_i32_dpp v63, v63, v63 row_shr:2 row_mask:0xf bank_mask:0xf
	v_fract_f32 v153, v153
	v_exp_f32 v152, v152
	v_max_i32_dpp v63, v63, v63 row_shr:4 row_mask:0xf bank_mask:0xf
	v_cos_f32 v184, v153
	v_sin_f32 v185, v153
	v_max_i32_dpp v63, v63, v63 row_shr:8 row_mask:0xf bank_mask:0xf
	v_fract_f32 v155, v155
	v_exp_f32 v154, v154
	v_max_i32_dpp v63, v63, v63 row_bcast:15 row_mask:0xa bank_mask:0xf
	v_cos_f32 v186, v155
	v_sin_f32 v187, v155
	v_max_i32_dpp v63, v63, v63 row_bcast:31 row_mask:0xc bank_mask:0xf
	v_pk_mul_f32 v[182:183], v[182:183], v[150:151] op_sel_hi:[1,0]
	v_pk_mul_f32 v[184:185], v[184:185], v[152:153] op_sel_hi:[1,0]
	v_pk_mul_f32 v[186:187], v[186:187], v[154:155] op_sel_hi:[1,0]
	v_readlane_b32 s42, v63, 63
	s_branch .Lk_task0
.Lk_sb:
	s_bfe_u32 s6, s37, 0x20003
	s_mul_i32 s42, s33, 163840
	s_lshl1_add_u32 s42, s35, s42
	v_mul_u32_u24 v31, 0x500, v1
	v_add_u32 v31, s42, v31
	v_mov_b64 v[6:7], 0
	s_mul_i32 s42, s33, 1088
	s_add_i32 s42, s42, 127824
	v_lshl_add_u32 v28, v1, 3, s42
	v_and_b32 v62, 0xff, v0
	v_lshlrev_b32 v29, 5, v62
	v_lshlrev_b32 v62, 4, v62
	v_add_u32 v252, 145232, v62
	v_and_b32 v62, 48, v1
	v_and_b32 v63, 3, v1
	v_lshl_add_u32 v62, v63, 2, v62
	v_mul_u32_u24 v55, 17312, v63
	v_bfe_u32 v63, v1, 2, 2
	v_lshl_add_u32 v62, v62, 1, v63
	v_lshl_add_u32 v54, v62, 3, s42
	s_lshl_b32 s43, s33, 8
	s_add_i32 s43, s43, 160
	v_and_b32 v63, 60, v1
	v_lshlrev_b32 v63, 2, v63
	v_add3_u32 v55, v55, v63, s43
	s_setprio 3
	v_mov_b32 v62, 0x3fb8aa3b
	s_waitcnt vmcnt(2)
	s_add_i32 s44, s6, 0
	s_and_b32 s44, s44, 3
	s_lshr_b32 s45, s44, 1
	s_mul_i32 s45, s45, 655360
	s_and_b32 s44, s44, 1
	s_mul_i32 s44, s44, 81920
	s_add_i32 s44, s44, s45
	s_add_u32 s44, s24, s44
	s_addc_u32 s45, s25, 0
	global_load_dwordx2 v[2:3], v31, s[44:45]
	v_mul_f32 v63, v59, v62
	v_exp_f32 v63, v63
	v_lshlrev_b32 v150, 16, v144
	v_and_b32 v152, s60, v144
	v_lshlrev_b32 v151, 16, v145
	v_and_b32 v153, s60, v145
	v_lshlrev_b32 v156, 16, v146
	v_and_b32 v158, s60, v146
	v_lshlrev_b32 v157, 16, v147
	v_and_b32 v159, s60, v147
	v_mul_f32 v150, v62, v150
	v_mul_f32 v152, v62, v152
	v_exp_f32 v150, v150
	v_exp_f32 v152, v152
	v_cvt_pk_bf16_f32 v63, v63, v63
	v_lshlrev_b32 v63, 16, v63
	v_mul_f32 v188, v62, v63
	v_mul_f32 v189, 0x3e22f983, v63
	v_readfirstlane_b32 s40, v63
	v_cvt_pk_bf16_f32 v150, v150, v152
	v_max3_f32 v63, |v156|, |v157|, |v158|
	v_and_b32 v152, s60, v150
	v_max3_f32 v63, v63, |v159|, |v159|
	v_lshlrev_b32 v150, 16, v150
	v_frexp_exp_i32_f32 v63, v63
	v_pk_mul_f32 v[150:151], v[150:151], v[188:189] neg_lo:[1,0]
	v_pk_mul_f32 v[152:153], v[152:153], v[188:189] neg_lo:[1,0]
	v_fract_f32 v151, v151
	v_exp_f32 v150, v150
	v_max_i32_dpp v63, v63, v63 row_shr:1 row_mask:0xf bank_mask:0xf
	v_cos_f32 v182, v151
	v_sin_f32 v183, v151
	v_max_i32_dpp v63, v63, v63 row_shr:2 row_mask:0xf bank_mask:0xf
	v_fract_f32 v153, v153
	v_exp_f32 v152, v152
	v_max_i32_dpp v63, v63, v63 row_shr:4 row_mask:0xf bank_mask:0xf
	v_cos_f32 v184, v153
	v_sin_f32 v185, v153
	v_max_i32_dpp v63, v63, v63 row_shr:8 row_mask:0xf bank_mask:0xf
	v_pk_mul_f32 v[182:183], v[182:183], v[150:151] op_sel_hi:[1,0]
	v_pk_mul_f32 v[184:185], v[184:185], v[152:153] op_sel_hi:[1,0]
	v_max_i32_dpp v63, v63, v63 row_bcast:15 row_mask:0xa bank_mask:0xf
	s_nop 1
	v_max_i32_dpp v63, v63, v63 row_bcast:31 row_mask:0xc bank_mask:0xf
	s_nop 0
	v_readlane_b32 s42, v63, 63
.Lk_task0:
	s_nop 0
	v_cvt_pk_f16_f32 v190, v182, v183
	s_max_i32 s42, s42, -100
	v_pk_mul_f32 v[146:147], v[182:183], v[182:183] op_sel_hi:[1,0]
	v_pk_fma_f32 v[182:183], v[182:183], v[182:183], v[146:147] op_sel:[1,1,0] op_sel_hi:[0,1,1] neg_lo:[1,0,0]
	s_min_i32 s42, s42, 110
	v_cvt_pk_f16_f32 v191, v182, v183
	s_cmp_eq_u32 s55, 0
	s_cbranch_scc0 .Lk_v00
	v_lshlrev_b32 v61, 4, v1
	global_load_dwordx4 v[32:35], v61, s[4:5] offset:0
	global_load_dwordx4 v[36:39], v61, s[4:5] offset:1024
	global_load_dwordx4 v[40:43], v61, s[4:5] offset:2048
	global_load_dwordx4 v[44:47], v61, s[4:5] offset:3072
	v_add_u32 v61, 0x1000, v61
	global_load_dwordx4 v[48:51], v61, s[4:5]
.Lk_v00:
	v_pk_mul_f32 v[146:147], v[182:183], v[182:183] op_sel_hi:[1,0]
	s_bfe_u32 s43, s40, 0x80017
	v_pk_fma_f32 v[182:183], v[182:183], v[182:183], v[146:147] op_sel:[1,1,0] op_sel_hi:[0,1,1] neg_lo:[1,0,0]
	s_add_i32 s43, s43, s42
	v_cvt_pk_f16_f32 v192, v182, v183
	v_pk_mul_f32 v[146:147], v[182:183], v[182:183] op_sel_hi:[1,0]
	s_add_i32 s43, s43, s34
	v_pk_fma_f32 v[182:183], v[182:183], v[182:183], v[146:147] op_sel:[1,1,0] op_sel_hi:[0,1,1] neg_lo:[1,0,0]
	s_add_i32 s43, s43, -13
	v_cvt_pk_f16_f32 v193, v182, v183
	v_pk_mul_f32 v[146:147], v[182:183], v[182:183] op_sel_hi:[1,0]
	s_max_i32 s43, s43, 0
	v_pk_fma_f32 v[182:183], v[182:183], v[182:183], v[146:147] op_sel:[1,1,0] op_sel_hi:[0,1,1] neg_lo:[1,0,0]
	v_cvt_pk_f16_f32 v194, v182, v183
	s_min_i32 s43, s43, 254
	v_pk_mul_f32 v[146:147], v[182:183], v[182:183] op_sel_hi:[1,0]
	s_lshl_b32 s48, s43, 23
	v_pk_fma_f32 v[182:183], v[182:183], v[182:183], v[146:147] op_sel:[1,1,0] op_sel_hi:[0,1,1] neg_lo:[1,0,0]
	v_cvt_pk_f16_f32 v195, v182, v183
	s_cmp_eq_u32 s55, 1
	s_cbranch_scc0 .Lk_v01
	s_add_i32 s44, s6, 1
	s_and_b32 s44, s44, 3
	s_lshr_b32 s45, s44, 1
	s_mul_i32 s45, s45, 655360
	s_and_b32 s44, s44, 1
	s_mul_i32 s44, s44, 81920
	s_add_i32 s44, s44, s45
	s_add_u32 s44, s24, s44
	s_addc_u32 s45, s25, 0
	global_load_dwordx2 v[4:5], v31, s[44:45]
	s_cmp_eq_u32 s33, 0
	s_cbranch_scc1 .Lk_v01
	s_add_u32 s44, s24, 0xffffd800
	s_addc_u32 s45, s25, -1
	s_mov_b64 exec, 0xff
	global_load_dwordx2 v[6:7], v31, s[44:45]
	s_mov_b64 exec, -1
.Lk_v01:
	s_cmp_eq_u32 s40, 0
	v_pk_mul_f32 v[146:147], v[182:183], v[182:183] op_sel_hi:[1,0]
	s_cselect_b32 s48, 0, s48
	v_pk_fma_f32 v[182:183], v[182:183], v[182:183], v[146:147] op_sel:[1,1,0] op_sel_hi:[0,1,1] neg_lo:[1,0,0]
	v_cvt_pk_f16_f32 v196, v182, v183
	s_sub_i32 s42, 140, s42
	v_pk_mul_f32 v[146:147], v[182:183], v[182:183] op_sel_hi:[1,0]
	v_pk_fma_f32 v[182:183], v[182:183], v[182:183], v[146:147] op_sel:[1,1,0] op_sel_hi:[0,1,1] neg_lo:[1,0,0]
	s_lshl_b32 s42, s42, 23
	v_cvt_pk_f16_f32 v197, v182, v183
	s_and_b32 s46, s40, 0x807fffff
	v_pk_mul_f32 v[146:147], v[182:183], v[182:183] op_sel_hi:[1,0]
	v_pk_fma_f32 v[182:183], v[182:183], v[182:183], v[146:147] op_sel:[1,1,0] op_sel_hi:[0,1,1] neg_lo:[1,0,0]
	s_or_b32 s46, s46, s42
	v_cvt_pk_f16_f32 v198, v182, v183
	s_xor_b32 s47, s46, 0x80000000
	v_pk_mul_f32 v[146:147], v[182:183], v[182:183] op_sel_hi:[1,0]
	v_pk_fma_f32 v[182:183], v[182:183], v[182:183], v[146:147] op_sel:[1,1,0] op_sel_hi:[0,1,1] neg_lo:[1,0,0]
	v_cvt_pk_f16_f32 v199, v182, v183
	v_pk_mul_f32 v[148:149], v[156:157], s[46:47]
	v_cvt_pk_f16_f32 v217, v148, v149
	v_pk_mul_f16 v204, v193, v194 op_sel_hi:[1,0]
	v_pk_fma_f16 v204, v193, v194, v204 op_sel:[1,1,0] op_sel_hi:[0,1,1] neg_lo:[1,0,0]
	v_pk_mul_f16 v206, v193, v195 op_sel_hi:[1,0]
	v_pk_fma_f16 v206, v193, v195, v206 op_sel:[1,1,0] op_sel_hi:[0,1,1] neg_lo:[1,0,0]
	s_mov_b32 m0, s50
	s_nop 0
	ds_write_addtid_b32 v193 offset:0
	ds_write_addtid_b32 v194 offset:2064
	ds_write_addtid_b32 v204 offset:4128
	ds_write_addtid_b32 v195 offset:6192
	v_pk_mul_f16 v207, v194, v195 op_sel_hi:[1,0]
	v_pk_fma_f16 v207, v194, v195, v207 op_sel:[1,1,0] op_sel_hi:[0,1,1] neg_lo:[1,0,0]
	v_pk_mul_f16 v208, v204, v195 op_sel_hi:[1,0]
	v_pk_fma_f16 v208, v204, v195, v208 op_sel:[1,1,0] op_sel_hi:[0,1,1] neg_lo:[1,0,0]
	ds_write_addtid_b32 v206 offset:8256
	ds_write_addtid_b32 v207 offset:10320
	v_pk_mul_f16 v210, v193, v196 op_sel_hi:[1,0]
	v_pk_fma_f16 v210, v193, v196, v210 op_sel:[1,1,0] op_sel_hi:[0,1,1] neg_lo:[1,0,0]
	v_pk_mul_f16 v211, v194, v196 op_sel_hi:[1,0]
	v_pk_fma_f16 v211, v194, v196, v211 op_sel:[1,1,0] op_sel_hi:[0,1,1] neg_lo:[1,0,0]
	ds_write_addtid_b32 v208 offset:12384
	ds_write_addtid_b32 v196 offset:14448
	ds_write_addtid_b32 v210 offset:16512
	v_pk_mul_f16 v212, v204, v196 op_sel_hi:[1,0]
	v_pk_fma_f16 v212, v204, v196, v212 op_sel:[1,1,0] op_sel_hi:[0,1,1] neg_lo:[1,0,0]
	v_pk_mul_f16 v213, v195, v196 op_sel_hi:[1,0]
	v_pk_fma_f16 v213, v195, v196, v213 op_sel:[1,1,0] op_sel_hi:[0,1,1] neg_lo:[1,0,0]
	ds_write_addtid_b32 v211 offset:18576
	ds_write_addtid_b32 v212 offset:20640
	v_pk_mul_f16 v214, v206, v196 op_sel_hi:[1,0]
	v_pk_fma_f16 v214, v206, v196, v214 op_sel:[1,1,0] op_sel_hi:[0,1,1] neg_lo:[1,0,0]
	s_cmp_eq_u32 s55, 1
	s_cbranch_scc0 .Lk_v02
	s_add_i32 s44, s6, 2
	s_and_b32 s44, s44, 3
	s_lshr_b32 s45, s44, 1
	s_mul_i32 s45, s45, 655360
	s_and_b32 s44, s44, 1
	s_mul_i32 s44, s44, 81920
	s_add_i32 s44, s44, s45
	s_add_u32 s44, s24, s44
	s_addc_u32 s45, s25, 0
	global_load_dwordx2 v[8:9], v31, s[44:45]
.Lk_v02:
	v_pk_mul_f16 v215, v207, v196 op_sel_hi:[1,0]
	v_pk_fma_f16 v215, v207, v196, v215 op_sel:[1,1,0] op_sel_hi:[0,1,1] neg_lo:[1,0,0]
	ds_write_addtid_b32 v213 offset:22704
	ds_write_addtid_b32 v214 offset:24768
	v_pk_mul_f16 v216, v208, v196 op_sel_hi:[1,0]
	v_pk_fma_f16 v216, v208, v196, v216 op_sel:[1,1,0] op_sel_hi:[0,1,1] neg_lo:[1,0,0]
	v_pk_mul_f16 v231, v193, v197 op_sel_hi:[1,0]
	v_pk_fma_f16 v231, v193, v197, v231 op_sel:[1,1,0] op_sel_hi:[0,1,1] neg_lo:[1,0,0]
	ds_write_addtid_b32 v215 offset:26832
	ds_write_addtid_b32 v216 offset:28896
	ds_write_addtid_b32 v197 offset:30960
	v_pk_mul_f16 v232, v194, v197 op_sel_hi:[1,0]
	v_pk_fma_f16 v232, v194, v197, v232 op_sel:[1,1,0] op_sel_hi:[0,1,1] neg_lo:[1,0,0]
	v_pk_mul_f16 v233, v204, v197 op_sel_hi:[1,0]
	v_pk_fma_f16 v233, v204, v197, v233 op_sel:[1,1,0] op_sel_hi:[0,1,1] neg_lo:[1,0,0]
	ds_write_addtid_b32 v231 offset:33024
	ds_write_addtid_b32 v232 offset:35088
	v_pk_mul_f16 v234, v195, v197 op_sel_hi:[1,0]
	v_pk_fma_f16 v234, v195, v197, v234 op_sel:[1,1,0] op_sel_hi:[0,1,1] neg_lo:[1,0,0]
	v_pk_mul_f16 v235, v206, v197 op_sel_hi:[1,0]
	v_pk_fma_f16 v235, v206, v197, v235 op_sel:[1,1,0] op_sel_hi:[0,1,1] neg_lo:[1,0,0]
	ds_write_addtid_b32 v233 offset:37152
	ds_write_addtid_b32 v234 offset:39216
	v_pk_mul_f16 v236, v207, v197 op_sel_hi:[1,0]
	v_pk_fma_f16 v236, v207, v197, v236 op_sel:[1,1,0] op_sel_hi:[0,1,1] neg_lo:[1,0,0]
	v_pk_mul_f16 v237, v208, v197 op_sel_hi:[1,0]
	v_pk_fma_f16 v237, v208, v197, v237 op_sel:[1,1,0] op_sel_hi:[0,1,1] neg_lo:[1,0,0]
	ds_write_addtid_b32 v235 offset:41280
	ds_write_addtid_b32 v236 offset:43344
	v_pk_mul_f16 v238, v196, v197 op_sel_hi:[1,0]
	v_pk_fma_f16 v238, v196, v197, v238 op_sel:[1,1,0] op_sel_hi:[0,1,1] neg_lo:[1,0,0]
	v_pk_mul_f16 v239, v210, v197 op_sel_hi:[1,0]
	v_pk_fma_f16 v239, v210, v197, v239 op_sel:[1,1,0] op_sel_hi:[0,1,1] neg_lo:[1,0,0]
	ds_write_addtid_b32 v237 offset:45408
	ds_write_addtid_b32 v238 offset:47472
	v_pk_mul_f16 v230, v211, v197 op_sel_hi:[1,0]
	v_pk_fma_f16 v230, v211, v197, v230 op_sel:[1,1,0] op_sel_hi:[0,1,1] neg_lo:[1,0,0]
	v_pk_mul_f16 v231, v212, v197 op_sel_hi:[1,0]
	v_pk_fma_f16 v231, v212, v197, v231 op_sel:[1,1,0] op_sel_hi:[0,1,1] neg_lo:[1,0,0]
	ds_write_addtid_b32 v239 offset:49536
	ds_write_addtid_b32 v230 offset:51600
	v_pk_mul_f16 v232, v213, v197 op_sel_hi:[1,0]
	v_pk_fma_f16 v232, v213, v197, v232 op_sel:[1,1,0] op_sel_hi:[0,1,1] neg_lo:[1,0,0]
	v_pk_mul_f16 v233, v214, v197 op_sel_hi:[1,0]
	v_pk_fma_f16 v233, v214, v197, v233 op_sel:[1,1,0] op_sel_hi:[0,1,1] neg_lo:[1,0,0]
	ds_write_addtid_b32 v231 offset:53664
	ds_write_addtid_b32 v232 offset:55728
	v_pk_mul_f16 v234, v215, v197 op_sel_hi:[1,0]
	v_pk_fma_f16 v234, v215, v197, v234 op_sel:[1,1,0] op_sel_hi:[0,1,1] neg_lo:[1,0,0]
	v_pk_mul_f16 v235, v216, v197 op_sel_hi:[1,0]
	v_pk_fma_f16 v235, v216, v197, v235 op_sel:[1,1,0] op_sel_hi:[0,1,1] neg_lo:[1,0,0]
	s_cmp_eq_u32 s55, 1
	s_cbranch_scc0 .Lk_v03
	s_add_i32 s44, s6, 3
	s_and_b32 s44, s44, 3
	s_lshr_b32 s45, s44, 1
	s_mul_i32 s45, s45, 655360
	s_and_b32 s44, s44, 1
	s_mul_i32 s44, s44, 81920
	s_add_i32 s44, s44, s45
	s_add_u32 s44, s24, s44
	s_addc_u32 s45, s25, 0
	global_load_dwordx2 v[10:11], v31, s[44:45]
	s_add_u32 s44, s24, 0x9d800
	s_addc_u32 s45, s25, 0
	s_mov_b64 exec, 0xff
	global_load_dwordx2 v[12:13], v31, s[44:45]
	s_mov_b64 exec, -1
.Lk_v03:
	ds_write_addtid_b32 v233 offset:57792
	ds_write_addtid_b32 v234 offset:59856
	v_pk_mul_f16 v218, v217, v190 op_sel_hi:[1,0]
	v_pk_fma_f16 v218, v217, v190, v218 op_sel:[1,1,0] op_sel_hi:[0,1,1] neg_hi:[1,0,0]
	v_pk_mul_f16 v219, v217, v191 op_sel_hi:[1,0]
	v_pk_fma_f16 v219, v217, v191, v219 op_sel:[1,1,0] op_sel_hi:[0,1,1] neg_hi:[1,0,0]
	ds_write_addtid_b32 v235 offset:61920
	s_mov_b32 m0, s49
	s_nop 0
	ds_write_addtid_b32 v217 offset:0
	ds_write_addtid_b32 v218 offset:2064
	ds_read_b128 v[112:115], v30 offset:43344
	ds_read_b128 v[116:119], v30 offset:43376
	v_pk_mul_f16 v220, v218, v191 op_sel_hi:[1,0]
	v_pk_fma_f16 v220, v218, v191, v220 op_sel:[1,1,0] op_sel_hi:[0,1,1] neg_hi:[1,0,0]
	v_pk_mul_f16 v221, v217, v192 op_sel_hi:[1,0]
	v_pk_fma_f16 v221, v217, v192, v221 op_sel:[1,1,0] op_sel_hi:[0,1,1] neg_hi:[1,0,0]
	ds_write_addtid_b32 v219 offset:4128
	ds_write_addtid_b32 v220 offset:6192
	v_pk_mul_f16 v222, v218, v192 op_sel_hi:[1,0]
	v_pk_fma_f16 v222, v218, v192, v222 op_sel:[1,1,0] op_sel_hi:[0,1,1] neg_hi:[1,0,0]
	v_pk_mul_f16 v223, v219, v192 op_sel_hi:[1,0]
	v_pk_fma_f16 v223, v219, v192, v223 op_sel:[1,1,0] op_sel_hi:[0,1,1] neg_hi:[1,0,0]
	ds_write_addtid_b32 v221 offset:8256
	ds_write_addtid_b32 v222 offset:10320
	v_pk_mul_f16 v224, v220, v192 op_sel_hi:[1,0]
	v_pk_fma_f16 v224, v220, v192, v224 op_sel:[1,1,0] op_sel_hi:[0,1,1] neg_hi:[1,0,0]
	v_pk_mul_f16 v225, v217, v198 op_sel_hi:[1,0]
	v_pk_fma_f16 v225, v217, v198, v225 op_sel:[1,1,0] op_sel_hi:[0,1,1] neg_hi:[1,0,0]
	ds_write_addtid_b32 v223 offset:12384
	ds_read_b128 v[120:123], v30 offset:43408
	ds_read_b128 v[124:127], v30 offset:43440
	ds_write_addtid_b32 v224 offset:14448
	v_pk_mul_f16 v226, v217, v199 op_sel_hi:[1,0]
	v_pk_fma_f16 v226, v217, v199, v226 op_sel:[1,1,0] op_sel_hi:[0,1,1] neg_hi:[1,0,0]
	v_pk_mul_f16 v227, v225, v199 op_sel_hi:[1,0]
	v_pk_fma_f16 v227, v225, v199, v227 op_sel:[1,1,0] op_sel_hi:[0,1,1] neg_hi:[1,0,0]
	ds_write_addtid_b32 v225 offset:16512
	ds_write_addtid_b32 v226 offset:24768
	v_pk_mul_f16 v228, v226, v199 op_sel_hi:[1,0]
	v_pk_fma_f16 v228, v226, v199, v228 op_sel:[1,1,0] op_sel_hi:[0,1,1] neg_hi:[1,0,0]
	v_pk_mul_f16 v229, v225, v191 op_sel_hi:[1,0]
	v_pk_fma_f16 v229, v225, v191, v229 op_sel:[1,1,0] op_sel_hi:[0,1,1] neg_hi:[1,0,0]
	ds_write_addtid_b32 v227 offset:28896
	ds_write_addtid_b32 v228 offset:33024
	v_pk_mul_f16 v204, v227, v199 op_sel_hi:[1,0]
	v_pk_fma_f16 v204, v227, v199, v204 op_sel:[1,1,0] op_sel_hi:[0,1,1] neg_hi:[1,0,0]
	v_pk_mul_f16 v206, v228, v199 op_sel_hi:[1,0]
	v_pk_fma_f16 v206, v228, v199, v206 op_sel:[1,1,0] op_sel_hi:[0,1,1] neg_hi:[1,0,0]
	ds_read_b128 v[128:131], v30 offset:43472
	ds_read_b128 v[132:135], v30 offset:43504
	ds_write_addtid_b32 v229 offset:20640
	ds_write_addtid_b32 v204 offset:35088
	v_pk_mul_f16 v236, v225, v192 op_sel_hi:[1,0]
	s_cmp_eq_u32 s55, 1
	s_cbranch_scc0 .Lk_v04
	s_add_i32 s44, s6, 0
	s_and_b32 s44, s44, 3
	s_lshr_b32 s45, s44, 1
	s_mul_i32 s45, s45, 655360
	s_and_b32 s44, s44, 1
	s_mul_i32 s44, s44, 81920
	s_add_i32 s44, s44, s45
	s_add_i32 s44, s44, 1310720
	s_add_u32 s44, s24, s44
	s_addc_u32 s45, s25, 0
	global_load_dwordx2 v[14:15], v31, s[44:45]
.Lk_v04:
	v_pk_fma_f16 v236, v225, v192, v236 op_sel:[1,1,0] op_sel_hi:[0,1,1] neg_hi:[1,0,0]
	v_pk_mul_f16 v237, v226, v192 op_sel_hi:[1,0]
	v_pk_fma_f16 v237, v226, v192, v237 op_sel:[1,1,0] op_sel_hi:[0,1,1] neg_hi:[1,0,0]
	ds_write_addtid_b32 v206 offset:37152
	ds_write_addtid_b32 v236 offset:18576
	v_pk_mul_f16 v238, v227, v192 op_sel_hi:[1,0]
	v_pk_fma_f16 v238, v227, v192, v238 op_sel:[1,1,0] op_sel_hi:[0,1,1] neg_hi:[1,0,0]
	v_pk_mul_f16 v239, v229, v192 op_sel_hi:[1,0]
	v_pk_fma_f16 v239, v229, v192, v239 op_sel:[1,1,0] op_sel_hi:[0,1,1] neg_hi:[1,0,0]
	ds_write_addtid_b32 v237 offset:26832
	ds_write_addtid_b32 v238 offset:30960
	v_pk_mul_f16 v230, v204, v199 op_sel_hi:[1,0]
	v_pk_fma_f16 v230, v204, v199, v230 op_sel:[1,1,0] op_sel_hi:[0,1,1] neg_hi:[1,0,0]
	ds_read_b128 v[136:139], v30 offset:43536
	ds_read_b128 v[140:143], v30 offset:43568
	v_pk_mul_f16 v231, v206, v199 op_sel_hi:[1,0]
	v_pk_fma_f16 v231, v206, v199, v231 op_sel:[1,1,0] op_sel_hi:[0,1,1] neg_hi:[1,0,0]
	ds_write_addtid_b32 v239 offset:22704
	ds_write_addtid_b32 v230 offset:39216
	ds_write_addtid_b32 v231 offset:41280
	s_cmp_eq_u32 s55, 2
	s_cbranch_scc1 .Lk_htail
	s_setprio 2
	v_cvt_pk_f16_f32 v190, v184, v185
	ds_read_b128 v[80:83], v30 offset:0
	ds_read_b128 v[84:87], v30 offset:32
	v_pk_mul_f32 v[146:147], v[184:185], v[184:185] op_sel_hi:[1,0]
	v_pk_fma_f32 v[184:185], v[184:185], v[184:185], v[146:147] op_sel:[1,1,0] op_sel_hi:[0,1,1] neg_lo:[1,0,0]
	v_cvt_pk_f16_f32 v191, v184, v185
	v_pk_mul_f32 v[146:147], v[184:185], v[184:185] op_sel_hi:[1,0]
	v_pk_fma_f32 v[184:185], v[184:185], v[184:185], v[146:147] op_sel:[1,1,0] op_sel_hi:[0,1,1] neg_lo:[1,0,0]
	v_cvt_pk_f16_f32 v192, v184, v185
	v_pk_mul_f32 v[146:147], v[184:185], v[184:185] op_sel_hi:[1,0]
	v_pk_fma_f32 v[184:185], v[184:185], v[184:185], v[146:147] op_sel:[1,1,0] op_sel_hi:[0,1,1] neg_lo:[1,0,0]
	ds_read_b128 v[88:91], v30 offset:64
	ds_read_b128 v[92:95], v30 offset:96
	v_cvt_pk_f16_f32 v193, v184, v185
	v_pk_mul_f32 v[146:147], v[184:185], v[184:185] op_sel_hi:[1,0]
	v_pk_fma_f32 v[184:185], v[184:185], v[184:185], v[146:147] op_sel:[1,1,0] op_sel_hi:[0,1,1] neg_lo:[1,0,0]
	v_cvt_pk_f16_f32 v194, v184, v185
	v_pk_mul_f32 v[146:147], v[184:185], v[184:185] op_sel_hi:[1,0]
	v_pk_fma_f32 v[184:185], v[184:185], v[184:185], v[146:147] op_sel:[1,1,0] op_sel_hi:[0,1,1] neg_lo:[1,0,0]
	v_cvt_pk_f16_f32 v195, v184, v185
	v_pk_mul_f32 v[146:147], v[184:185], v[184:185] op_sel_hi:[1,0]
	ds_read_b128 v[96:99], v30 offset:128
	ds_read_b128 v[100:103], v30 offset:160
	v_pk_fma_f32 v[184:185], v[184:185], v[184:185], v[146:147] op_sel:[1,1,0] op_sel_hi:[0,1,1] neg_lo:[1,0,0]
	s_cmp_eq_u32 s55, 1
	s_cbranch_scc0 .Lk_v11
	s_add_i32 s44, s6, 1
	s_and_b32 s44, s44, 3
	s_lshr_b32 s45, s44, 1
	s_mul_i32 s45, s45, 655360
	s_and_b32 s44, s44, 1
	s_mul_i32 s44, s44, 81920
	s_add_i32 s44, s44, s45
	s_add_i32 s44, s44, 1310720
	s_add_u32 s44, s24, s44
	s_addc_u32 s45, s25, 0
	global_load_dwordx2 v[16:17], v31, s[44:45]
	s_add_u32 s44, s24, 0x13d800
	s_addc_u32 s45, s25, 0
	s_mov_b64 exec, 0xff
	global_load_dwordx2 v[18:19], v31, s[44:45]
	s_mov_b64 exec, -1
.Lk_v11:
	v_cvt_pk_f16_f32 v196, v184, v185
	v_pk_mul_f32 v[146:147], v[184:185], v[184:185] op_sel_hi:[1,0]
	v_pk_fma_f32 v[184:185], v[184:185], v[184:185], v[146:147] op_sel:[1,1,0] op_sel_hi:[0,1,1] neg_lo:[1,0,0]
	v_cvt_pk_f16_f32 v197, v184, v185
	v_pk_mul_f32 v[146:147], v[184:185], v[184:185] op_sel_hi:[1,0]
	v_pk_fma_f32 v[184:185], v[184:185], v[184:185], v[146:147] op_sel:[1,1,0] op_sel_hi:[0,1,1] neg_lo:[1,0,0]
	v_cvt_pk_f16_f32 v198, v184, v185
	ds_read_b128 v[104:107], v30 offset:192
	ds_read_b128 v[108:111], v30 offset:224
	v_pk_mul_f32 v[146:147], v[184:185], v[184:185] op_sel_hi:[1,0]
	v_pk_fma_f32 v[184:185], v[184:185], v[184:185], v[146:147] op_sel:[1,1,0] op_sel_hi:[0,1,1] neg_lo:[1,0,0]
	v_cvt_pk_f16_f32 v199, v184, v185
	v_pk_mul_f32 v[148:149], v[158:159], s[46:47]
	v_cvt_pk_f16_f32 v217, v148, v149
	v_pk_mul_f16 v204, v193, v194 op_sel_hi:[1,0]
	v_pk_fma_f16 v204, v193, v194, v204 op_sel:[1,1,0] op_sel_hi:[0,1,1] neg_lo:[1,0,0]
	v_pk_mul_f16 v206, v193, v195 op_sel_hi:[1,0]
	v_pk_fma_f16 v206, v193, v195, v206 op_sel:[1,1,0] op_sel_hi:[0,1,1] neg_lo:[1,0,0]
	s_mov_b32 m0, s50
	s_waitcnt lgkmcnt(7)
	v_mfma_f32_32x32x16_f16 v[64:79], v[80:83], v[112:115], 0
	ds_write_addtid_b32 v193 offset:0
	ds_write_addtid_b32 v194 offset:2064
	ds_write_addtid_b32 v204 offset:4128
	ds_write_addtid_b32 v195 offset:6192
	v_pk_mul_f16 v207, v194, v195 op_sel_hi:[1,0]
	v_pk_fma_f16 v207, v194, v195, v207 op_sel:[1,1,0] op_sel_hi:[0,1,1] neg_lo:[1,0,0]
	v_pk_mul_f16 v208, v204, v195 op_sel_hi:[1,0]
	v_pk_fma_f16 v208, v204, v195, v208 op_sel:[1,1,0] op_sel_hi:[0,1,1] neg_lo:[1,0,0]
	ds_write_addtid_b32 v206 offset:8256
	ds_write_addtid_b32 v207 offset:10320
	v_pk_mul_f16 v210, v193, v196 op_sel_hi:[1,0]
	v_pk_fma_f16 v210, v193, v196, v210 op_sel:[1,1,0] op_sel_hi:[0,1,1] neg_lo:[1,0,0]
	s_waitcnt lgkmcnt(12)
	v_mfma_f32_32x32x16_f16 v[64:79], v[84:87], v[116:119], v[64:79]
	v_pk_mul_f16 v211, v194, v196 op_sel_hi:[1,0]
	v_pk_fma_f16 v211, v194, v196, v211 op_sel:[1,1,0] op_sel_hi:[0,1,1] neg_lo:[1,0,0]
	ds_write_addtid_b32 v208 offset:12384
	ds_write_addtid_b32 v196 offset:14448
	ds_write_addtid_b32 v210 offset:16512
	v_pk_mul_f16 v212, v204, v196 op_sel_hi:[1,0]
	v_pk_fma_f16 v212, v204, v196, v212 op_sel:[1,1,0] op_sel_hi:[0,1,1] neg_lo:[1,0,0]
	v_pk_mul_f16 v213, v195, v196 op_sel_hi:[1,0]
	v_pk_fma_f16 v213, v195, v196, v213 op_sel:[1,1,0] op_sel_hi:[0,1,1] neg_lo:[1,0,0]
	s_cmp_eq_u32 s55, 1
	s_cbranch_scc0 .Lk_v12
	s_add_i32 s44, s6, 2
	s_and_b32 s44, s44, 3
	s_lshr_b32 s45, s44, 1
	s_mul_i32 s45, s45, 655360
	s_and_b32 s44, s44, 1
	s_mul_i32 s44, s44, 81920
	s_add_i32 s44, s44, s45
	s_add_i32 s44, s44, 1310720
	s_add_u32 s44, s24, s44
	s_addc_u32 s45, s25, 0
	global_load_dwordx2 v[20:21], v31, s[44:45]
.Lk_v12:
	ds_write_addtid_b32 v211 offset:18576
	ds_write_addtid_b32 v212 offset:20640
	s_waitcnt lgkmcnt(14)
	v_mfma_f32_32x32x16_f16 v[64:79], v[88:91], v[120:123], v[64:79]
	v_pk_mul_f16 v214, v206, v196 op_sel_hi:[1,0]
	v_pk_fma_f16 v214, v206, v196, v214 op_sel:[1,1,0] op_sel_hi:[0,1,1] neg_lo:[1,0,0]
	v_pk_mul_f16 v215, v207, v196 op_sel_hi:[1,0]
	v_pk_fma_f16 v215, v207, v196, v215 op_sel:[1,1,0] op_sel_hi:[0,1,1] neg_lo:[1,0,0]
	ds_write_addtid_b32 v213 offset:22704
	ds_write_addtid_b32 v214 offset:24768
	v_pk_mul_f16 v216, v208, v196 op_sel_hi:[1,0]
	v_pk_fma_f16 v216, v208, v196, v216 op_sel:[1,1,0] op_sel_hi:[0,1,1] neg_lo:[1,0,0]
	v_pk_mul_f16 v231, v193, v197 op_sel_hi:[1,0]
	v_pk_fma_f16 v231, v193, v197, v231 op_sel:[1,1,0] op_sel_hi:[0,1,1] neg_lo:[1,0,0]
	ds_write_addtid_b32 v215 offset:26832
	ds_write_addtid_b32 v216 offset:28896
	s_waitcnt lgkmcnt(14)
	v_mfma_f32_32x32x16_f16 v[64:79], v[92:95], v[124:127], v[64:79]
	ds_write_addtid_b32 v197 offset:30960
	v_pk_mul_f16 v232, v194, v197 op_sel_hi:[1,0]
	v_pk_fma_f16 v232, v194, v197, v232 op_sel:[1,1,0] op_sel_hi:[0,1,1] neg_lo:[1,0,0]
	v_pk_mul_f16 v233, v204, v197 op_sel_hi:[1,0]
	v_pk_fma_f16 v233, v204, v197, v233 op_sel:[1,1,0] op_sel_hi:[0,1,1] neg_lo:[1,0,0]
	ds_write_addtid_b32 v231 offset:33024
	ds_write_addtid_b32 v232 offset:35088
	v_pk_mul_f16 v234, v195, v197 op_sel_hi:[1,0]
	v_pk_fma_f16 v234, v195, v197, v234 op_sel:[1,1,0] op_sel_hi:[0,1,1] neg_lo:[1,0,0]
	v_pk_mul_f16 v235, v206, v197 op_sel_hi:[1,0]
	v_pk_fma_f16 v235, v206, v197, v235 op_sel:[1,1,0] op_sel_hi:[0,1,1] neg_lo:[1,0,0]
	s_waitcnt lgkmcnt(14)
	v_mfma_f32_32x32x16_f16 v[64:79], v[96:99], v[128:131], v[64:79]
	ds_write_addtid_b32 v233 offset:37152
	ds_write_addtid_b32 v234 offset:39216
	v_pk_mul_f16 v236, v207, v197 op_sel_hi:[1,0]
	v_pk_fma_f16 v236, v207, v197, v236 op_sel:[1,1,0] op_sel_hi:[0,1,1] neg_lo:[1,0,0]
	v_pk_mul_f16 v237, v208, v197 op_sel_hi:[1,0]
	v_pk_fma_f16 v237, v208, v197, v237 op_sel:[1,1,0] op_sel_hi:[0,1,1] neg_lo:[1,0,0]
	ds_write_addtid_b32 v235 offset:41280
	ds_write_addtid_b32 v236 offset:43344
	v_pk_mul_f16 v238, v196, v197 op_sel_hi:[1,0]
	v_pk_fma_f16 v238, v196, v197, v238 op_sel:[1,1,0] op_sel_hi:[0,1,1] neg_lo:[1,0,0]
	v_pk_mul_f16 v239, v210, v197 op_sel_hi:[1,0]
	v_pk_fma_f16 v239, v210, v197, v239 op_sel:[1,1,0] op_sel_hi:[0,1,1] neg_lo:[1,0,0]
	s_waitcnt lgkmcnt(14)
	v_mfma_f32_32x32x16_f16 v[64:79], v[100:103], v[132:135], v[64:79]
	s_cmp_eq_u32 s55, 1
	s_cbranch_scc0 .Lk_v13
	s_add_i32 s44, s6, 3
	s_and_b32 s44, s44, 3
	s_lshr_b32 s45, s44, 1
	s_mul_i32 s45, s45, 655360
	s_and_b32 s44, s44, 1
	s_mul_i32 s44, s44, 81920
	s_add_i32 s44, s44, s45
	s_add_i32 s44, s44, 1310720
	s_add_u32 s44, s24, s44
	s_addc_u32 s45, s25, 0
	global_load_dwordx2 v[22:23], v31, s[44:45]
	s_add_u32 s44, s24, 0x1dd800
	s_addc_u32 s45, s25, 0
	s_mov_b64 exec, 0xff
	global_load_dwordx2 v[24:25], v31, s[44:45]
	s_mov_b64 exec, -1
.Lk_v13:
	ds_write_addtid_b32 v237 offset:45408
	ds_write_addtid_b32 v238 offset:47472
	v_pk_mul_f16 v230, v211, v197 op_sel_hi:[1,0]
	v_pk_fma_f16 v230, v211, v197, v230 op_sel:[1,1,0] op_sel_hi:[0,1,1] neg_lo:[1,0,0]
	v_pk_mul_f16 v231, v212, v197 op_sel_hi:[1,0]
	v_pk_fma_f16 v231, v212, v197, v231 op_sel:[1,1,0] op_sel_hi:[0,1,1] neg_lo:[1,0,0]
	ds_write_addtid_b32 v239 offset:49536
	ds_write_addtid_b32 v230 offset:51600
	v_pk_mul_f16 v232, v213, v197 op_sel_hi:[1,0]
	v_pk_fma_f16 v232, v213, v197, v232 op_sel:[1,1,0] op_sel_hi:[0,1,1] neg_lo:[1,0,0]
	v_pk_mul_f16 v233, v214, v197 op_sel_hi:[1,0]
	s_waitcnt lgkmcnt(14)
	v_mfma_f32_32x32x16_f16 v[64:79], v[104:107], v[136:139], v[64:79]
	v_pk_fma_f16 v233, v214, v197, v233 op_sel:[1,1,0] op_sel_hi:[0,1,1] neg_lo:[1,0,0]
	ds_write_addtid_b32 v231 offset:53664
	ds_write_addtid_b32 v232 offset:55728
	v_pk_mul_f16 v234, v215, v197 op_sel_hi:[1,0]
	v_pk_fma_f16 v234, v215, v197, v234 op_sel:[1,1,0] op_sel_hi:[0,1,1] neg_lo:[1,0,0]
	v_pk_mul_f16 v235, v216, v197 op_sel_hi:[1,0]
	v_pk_fma_f16 v235, v216, v197, v235 op_sel:[1,1,0] op_sel_hi:[0,1,1] neg_lo:[1,0,0]
	ds_write_addtid_b32 v233 offset:57792
	ds_write_addtid_b32 v234 offset:59856
	v_pk_mul_f16 v218, v217, v190 op_sel_hi:[1,0]
	v_pk_fma_f16 v218, v217, v190, v218 op_sel:[1,1,0] op_sel_hi:[0,1,1] neg_hi:[1,0,0]
	s_waitcnt lgkmcnt(14)
	v_mfma_f32_32x32x16_f16 v[64:79], v[108:111], v[140:143], v[64:79]
	v_pk_mul_f16 v219, v217, v191 op_sel_hi:[1,0]
	v_pk_fma_f16 v219, v217, v191, v219 op_sel:[1,1,0] op_sel_hi:[0,1,1] neg_hi:[1,0,0]
	ds_write_addtid_b32 v235 offset:61920
	s_mov_b32 m0, s49
	s_nop 0
	ds_write_addtid_b32 v217 offset:0
	ds_write_addtid_b32 v218 offset:2064
	ds_read_b128 v[112:115], v30 offset:43344
	ds_read_b128 v[116:119], v30 offset:43376
	v_pk_mul_f16 v220, v218, v191 op_sel_hi:[1,0]
	v_pk_fma_f16 v220, v218, v191, v220 op_sel:[1,1,0] op_sel_hi:[0,1,1] neg_hi:[1,0,0]
	v_pk_mul_f16 v221, v217, v192 op_sel_hi:[1,0]
	v_pk_fma_f16 v221, v217, v192, v221 op_sel:[1,1,0] op_sel_hi:[0,1,1] neg_hi:[1,0,0]
	ds_write_addtid_b32 v219 offset:4128
	ds_write_addtid_b32 v220 offset:6192
	v_pk_mul_f16 v222, v218, v192 op_sel_hi:[1,0]
	v_pk_fma_f16 v222, v218, v192, v222 op_sel:[1,1,0] op_sel_hi:[0,1,1] neg_hi:[1,0,0]
	v_pk_mul_f16 v223, v219, v192 op_sel_hi:[1,0]
	v_pk_fma_f16 v223, v219, v192, v223 op_sel:[1,1,0] op_sel_hi:[0,1,1] neg_hi:[1,0,0]
	ds_write_addtid_b32 v221 offset:8256
	ds_write_addtid_b32 v222 offset:10320
	v_pk_mul_f16 v224, v220, v192 op_sel_hi:[1,0]
	v_pk_fma_f16 v224, v220, v192, v224 op_sel:[1,1,0] op_sel_hi:[0,1,1] neg_hi:[1,0,0]
	v_pk_mul_f16 v225, v217, v198 op_sel_hi:[1,0]
	v_pk_fma_f16 v225, v217, v198, v225 op_sel:[1,1,0] op_sel_hi:[0,1,1] neg_hi:[1,0,0]
	ds_write_addtid_b32 v223 offset:12384
	ds_read_b128 v[120:123], v30 offset:43408
	ds_read_b128 v[124:127], v30 offset:43440
	ds_write_addtid_b32 v224 offset:14448
	v_pk_mul_f16 v226, v217, v199 op_sel_hi:[1,0]
	v_pk_fma_f16 v226, v217, v199, v226 op_sel:[1,1,0] op_sel_hi:[0,1,1] neg_hi:[1,0,0]
	v_pk_mul_f16 v227, v225, v199 op_sel_hi:[1,0]
	v_pk_fma_f16 v227, v225, v199, v227 op_sel:[1,1,0] op_sel_hi:[0,1,1] neg_hi:[1,0,0]
	ds_write_addtid_b32 v225 offset:16512
	ds_write_addtid_b32 v226 offset:24768
	v_pk_mul_f16 v228, v226, v199 op_sel_hi:[1,0]
	v_pk_fma_f16 v228, v226, v199, v228 op_sel:[1,1,0] op_sel_hi:[0,1,1] neg_hi:[1,0,0]
	v_pk_mul_f16 v229, v225, v191 op_sel_hi:[1,0]
	v_pk_fma_f16 v229, v225, v191, v229 op_sel:[1,1,0] op_sel_hi:[0,1,1] neg_hi:[1,0,0]
	ds_write_addtid_b32 v227 offset:28896
	ds_write_addtid_b32 v228 offset:33024
	v_pk_mul_f16 v204, v227, v199 op_sel_hi:[1,0]
	v_pk_fma_f16 v204, v227, v199, v204 op_sel:[1,1,0] op_sel_hi:[0,1,1] neg_hi:[1,0,0]
	v_pk_mul_f16 v206, v228, v199 op_sel_hi:[1,0]
	v_pk_fma_f16 v206, v228, v199, v206 op_sel:[1,1,0] op_sel_hi:[0,1,1] neg_hi:[1,0,0]
	ds_read_b128 v[128:131], v30 offset:43472
	ds_read_b128 v[132:135], v30 offset:43504
	ds_write_addtid_b32 v229 offset:20640
	ds_write_addtid_b32 v204 offset:35088
	v_pk_mul_f16 v236, v225, v192 op_sel_hi:[1,0]
	v_pk_fma_f16 v236, v225, v192, v236 op_sel:[1,1,0] op_sel_hi:[0,1,1] neg_hi:[1,0,0]
	v_pk_mul_f16 v237, v226, v192 op_sel_hi:[1,0]
	v_pk_fma_f16 v237, v226, v192, v237 op_sel:[1,1,0] op_sel_hi:[0,1,1] neg_hi:[1,0,0]
	ds_write_addtid_b32 v206 offset:37152
	ds_write_addtid_b32 v236 offset:18576
	v_pk_mul_f16 v238, v227, v192 op_sel_hi:[1,0]
	v_pk_fma_f16 v238, v227, v192, v238 op_sel:[1,1,0] op_sel_hi:[0,1,1] neg_hi:[1,0,0]
	v_pk_mul_f16 v239, v229, v192 op_sel_hi:[1,0]
	v_pk_fma_f16 v239, v229, v192, v239 op_sel:[1,1,0] op_sel_hi:[0,1,1] neg_hi:[1,0,0]
	ds_write_addtid_b32 v237 offset:26832
	ds_write_addtid_b32 v238 offset:30960
	v_pk_mul_f16 v230, v204, v199 op_sel_hi:[1,0]
	v_pk_fma_f16 v230, v204, v199, v230 op_sel:[1,1,0] op_sel_hi:[0,1,1] neg_hi:[1,0,0]
	ds_read_b128 v[136:139], v30 offset:43536
	ds_read_b128 v[140:143], v30 offset:43568
	v_pk_mul_f16 v231, v206, v199 op_sel_hi:[1,0]
	v_pk_fma_f16 v231, v206, v199, v231 op_sel:[1,1,0] op_sel_hi:[0,1,1] neg_hi:[1,0,0]
	ds_write_addtid_b32 v239 offset:22704
	ds_write_addtid_b32 v230 offset:39216
	ds_write_addtid_b32 v231 offset:41280
	s_cmp_eq_u32 s55, 0
	s_cbranch_scc0 .Lk_partner
	s_setprio 1
	v_cvt_pk_f16_f32 v190, v186, v187
	ds_read_b128 v[80:83], v30 offset:0
	ds_read_b128 v[84:87], v30 offset:32
	v_pk_mul_f32 v[146:147], v[186:187], v[186:187] op_sel_hi:[1,0]
	v_pk_fma_f32 v[186:187], v[186:187], v[186:187], v[146:147] op_sel:[1,1,0] op_sel_hi:[0,1,1] neg_lo:[1,0,0]
	v_cvt_pk_f16_f32 v191, v186, v187
	v_pk_mul_f32 v[146:147], v[186:187], v[186:187] op_sel_hi:[1,0]
	v_pk_fma_f32 v[186:187], v[186:187], v[186:187], v[146:147] op_sel:[1,1,0] op_sel_hi:[0,1,1] neg_lo:[1,0,0]
	v_cvt_pk_f16_f32 v192, v186, v187
	v_pk_mul_f32 v[146:147], v[186:187], v[186:187] op_sel_hi:[1,0]
	v_pk_fma_f32 v[186:187], v[186:187], v[186:187], v[146:147] op_sel:[1,1,0] op_sel_hi:[0,1,1] neg_lo:[1,0,0]
	ds_read_b128 v[88:91], v30 offset:64
	ds_read_b128 v[92:95], v30 offset:96
	v_cvt_pk_f16_f32 v193, v186, v187
	v_pk_mul_f32 v[146:147], v[186:187], v[186:187] op_sel_hi:[1,0]
	v_pk_fma_f32 v[186:187], v[186:187], v[186:187], v[146:147] op_sel:[1,1,0] op_sel_hi:[0,1,1] neg_lo:[1,0,0]
	v_cvt_pk_f16_f32 v194, v186, v187
	v_pk_mul_f32 v[146:147], v[186:187], v[186:187] op_sel_hi:[1,0]
	v_pk_fma_f32 v[186:187], v[186:187], v[186:187], v[146:147] op_sel:[1,1,0] op_sel_hi:[0,1,1] neg_lo:[1,0,0]
	v_cvt_pk_f16_f32 v195, v186, v187
	v_pk_mul_f32 v[146:147], v[186:187], v[186:187] op_sel_hi:[1,0]
	ds_read_b128 v[96:99], v30 offset:128
	ds_read_b128 v[100:103], v30 offset:160
	v_pk_fma_f32 v[186:187], v[186:187], v[186:187], v[146:147] op_sel:[1,1,0] op_sel_hi:[0,1,1] neg_lo:[1,0,0]
	v_cvt_pk_f16_f32 v196, v186, v187
	v_pk_mul_f32 v[146:147], v[186:187], v[186:187] op_sel_hi:[1,0]
	v_pk_fma_f32 v[186:187], v[186:187], v[186:187], v[146:147] op_sel:[1,1,0] op_sel_hi:[0,1,1] neg_lo:[1,0,0]
	v_cvt_pk_f16_f32 v197, v186, v187
	v_pk_mul_f32 v[146:147], v[186:187], v[186:187] op_sel_hi:[1,0]
	v_pk_fma_f32 v[186:187], v[186:187], v[186:187], v[146:147] op_sel:[1,1,0] op_sel_hi:[0,1,1] neg_lo:[1,0,0]
	v_cvt_pk_f16_f32 v198, v186, v187
	ds_read_b128 v[104:107], v30 offset:192
	ds_read_b128 v[108:111], v30 offset:224
	v_pk_mul_f32 v[146:147], v[186:187], v[186:187] op_sel_hi:[1,0]
	v_pk_fma_f32 v[186:187], v[186:187], v[186:187], v[146:147] op_sel:[1,1,0] op_sel_hi:[0,1,1] neg_lo:[1,0,0]
	v_cvt_pk_f16_f32 v199, v186, v187
	v_pk_mul_f32 v[148:149], v[160:161], s[46:47]
	v_cvt_pk_f16_f32 v217, v148, v149
	v_pk_mul_f16 v204, v193, v194 op_sel_hi:[1,0]
	v_pk_fma_f16 v204, v193, v194, v204 op_sel:[1,1,0] op_sel_hi:[0,1,1] neg_lo:[1,0,0]
	v_pk_mul_f16 v206, v193, v195 op_sel_hi:[1,0]
	v_pk_fma_f16 v206, v193, v195, v206 op_sel:[1,1,0] op_sel_hi:[0,1,1] neg_lo:[1,0,0]
	s_mov_b32 m0, s50
	s_waitcnt lgkmcnt(7)
	v_mfma_f32_32x32x16_f16 v[64:79], v[80:83], v[112:115], v[64:79]
	ds_write_addtid_b32 v193 offset:0
	ds_write_addtid_b32 v194 offset:2064
	ds_write_addtid_b32 v204 offset:4128
	ds_write_addtid_b32 v195 offset:6192
	v_pk_mul_f16 v207, v194, v195 op_sel_hi:[1,0]
	v_pk_fma_f16 v207, v194, v195, v207 op_sel:[1,1,0] op_sel_hi:[0,1,1] neg_lo:[1,0,0]
	v_pk_mul_f16 v208, v204, v195 op_sel_hi:[1,0]
	v_pk_fma_f16 v208, v204, v195, v208 op_sel:[1,1,0] op_sel_hi:[0,1,1] neg_lo:[1,0,0]
	ds_write_addtid_b32 v206 offset:8256
	ds_write_addtid_b32 v207 offset:10320
	v_pk_mul_f16 v210, v193, v196 op_sel_hi:[1,0]
	v_pk_fma_f16 v210, v193, v196, v210 op_sel:[1,1,0] op_sel_hi:[0,1,1] neg_lo:[1,0,0]
	s_waitcnt lgkmcnt(12)
	v_mfma_f32_32x32x16_f16 v[64:79], v[84:87], v[116:119], v[64:79]
	v_pk_mul_f16 v211, v194, v196 op_sel_hi:[1,0]
	v_pk_fma_f16 v211, v194, v196, v211 op_sel:[1,1,0] op_sel_hi:[0,1,1] neg_lo:[1,0,0]
	ds_write_addtid_b32 v208 offset:12384
	ds_write_addtid_b32 v196 offset:14448
	ds_write_addtid_b32 v210 offset:16512
	v_pk_mul_f16 v212, v204, v196 op_sel_hi:[1,0]
	v_pk_fma_f16 v212, v204, v196, v212 op_sel:[1,1,0] op_sel_hi:[0,1,1] neg_lo:[1,0,0]
	v_pk_mul_f16 v213, v195, v196 op_sel_hi:[1,0]
	v_pk_fma_f16 v213, v195, v196, v213 op_sel:[1,1,0] op_sel_hi:[0,1,1] neg_lo:[1,0,0]
	ds_write_addtid_b32 v211 offset:18576
	ds_write_addtid_b32 v212 offset:20640
	s_waitcnt lgkmcnt(14)
	v_mfma_f32_32x32x16_f16 v[64:79], v[88:91], v[120:123], v[64:79]
	v_pk_mul_f16 v214, v206, v196 op_sel_hi:[1,0]
	v_pk_fma_f16 v214, v206, v196, v214 op_sel:[1,1,0] op_sel_hi:[0,1,1] neg_lo:[1,0,0]
	v_pk_mul_f16 v215, v207, v196 op_sel_hi:[1,0]
	v_pk_fma_f16 v215, v207, v196, v215 op_sel:[1,1,0] op_sel_hi:[0,1,1] neg_lo:[1,0,0]
	ds_write_addtid_b32 v213 offset:22704
	ds_write_addtid_b32 v214 offset:24768
	v_pk_mul_f16 v216, v208, v196 op_sel_hi:[1,0]
	v_pk_fma_f16 v216, v208, v196, v216 op_sel:[1,1,0] op_sel_hi:[0,1,1] neg_lo:[1,0,0]
	v_pk_mul_f16 v231, v193, v197 op_sel_hi:[1,0]
	v_pk_fma_f16 v231, v193, v197, v231 op_sel:[1,1,0] op_sel_hi:[0,1,1] neg_lo:[1,0,0]
	ds_write_addtid_b32 v215 offset:26832
	ds_write_addtid_b32 v216 offset:28896
	s_waitcnt lgkmcnt(14)
	v_mfma_f32_32x32x16_f16 v[64:79], v[92:95], v[124:127], v[64:79]
	ds_write_addtid_b32 v197 offset:30960
	v_pk_mul_f16 v232, v194, v197 op_sel_hi:[1,0]
	v_pk_fma_f16 v232, v194, v197, v232 op_sel:[1,1,0] op_sel_hi:[0,1,1] neg_lo:[1,0,0]
	v_pk_mul_f16 v233, v204, v197 op_sel_hi:[1,0]
	v_pk_fma_f16 v233, v204, v197, v233 op_sel:[1,1,0] op_sel_hi:[0,1,1] neg_lo:[1,0,0]
	ds_write_addtid_b32 v231 offset:33024
	ds_write_addtid_b32 v232 offset:35088
	v_pk_mul_f16 v234, v195, v197 op_sel_hi:[1,0]
	v_pk_fma_f16 v234, v195, v197, v234 op_sel:[1,1,0] op_sel_hi:[0,1,1] neg_lo:[1,0,0]
	v_pk_mul_f16 v235, v206, v197 op_sel_hi:[1,0]
	v_pk_fma_f16 v235, v206, v197, v235 op_sel:[1,1,0] op_sel_hi:[0,1,1] neg_lo:[1,0,0]
	s_waitcnt lgkmcnt(14)
	v_mfma_f32_32x32x16_f16 v[64:79], v[96:99], v[128:131], v[64:79]
	ds_write_addtid_b32 v233 offset:37152
	ds_write_addtid_b32 v234 offset:39216
	v_pk_mul_f16 v236, v207, v197 op_sel_hi:[1,0]
	v_pk_fma_f16 v236, v207, v197, v236 op_sel:[1,1,0] op_sel_hi:[0,1,1] neg_lo:[1,0,0]
	v_pk_mul_f16 v237, v208, v197 op_sel_hi:[1,0]
	v_pk_fma_f16 v237, v208, v197, v237 op_sel:[1,1,0] op_sel_hi:[0,1,1] neg_lo:[1,0,0]
	ds_write_addtid_b32 v235 offset:41280
	ds_write_addtid_b32 v236 offset:43344
	v_pk_mul_f16 v238, v196, v197 op_sel_hi:[1,0]
	v_pk_fma_f16 v238, v196, v197, v238 op_sel:[1,1,0] op_sel_hi:[0,1,1] neg_lo:[1,0,0]
	v_pk_mul_f16 v239, v210, v197 op_sel_hi:[1,0]
	v_pk_fma_f16 v239, v210, v197, v239 op_sel:[1,1,0] op_sel_hi:[0,1,1] neg_lo:[1,0,0]
	s_waitcnt lgkmcnt(14)
	v_mfma_f32_32x32x16_f16 v[64:79], v[100:103], v[132:135], v[64:79]
	ds_write_addtid_b32 v237 offset:45408
	ds_write_addtid_b32 v238 offset:47472
	v_pk_mul_f16 v230, v211, v197 op_sel_hi:[1,0]
	v_pk_fma_f16 v230, v211, v197, v230 op_sel:[1,1,0] op_sel_hi:[0,1,1] neg_lo:[1,0,0]
	v_pk_mul_f16 v231, v212, v197 op_sel_hi:[1,0]
	v_pk_fma_f16 v231, v212, v197, v231 op_sel:[1,1,0] op_sel_hi:[0,1,1] neg_lo:[1,0,0]
	ds_write_addtid_b32 v239 offset:49536
	ds_write_addtid_b32 v230 offset:51600
	v_pk_mul_f16 v232, v213, v197 op_sel_hi:[1,0]
	v_pk_fma_f16 v232, v213, v197, v232 op_sel:[1,1,0] op_sel_hi:[0,1,1] neg_lo:[1,0,0]
	v_pk_mul_f16 v233, v214, v197 op_sel_hi:[1,0]
	s_waitcnt lgkmcnt(14)
	v_mfma_f32_32x32x16_f16 v[64:79], v[104:107], v[136:139], v[64:79]
	v_pk_fma_f16 v233, v214, v197, v233 op_sel:[1,1,0] op_sel_hi:[0,1,1] neg_lo:[1,0,0]
	ds_write_addtid_b32 v231 offset:53664
	ds_write_addtid_b32 v232 offset:55728
	v_pk_mul_f16 v234, v215, v197 op_sel_hi:[1,0]
	v_pk_fma_f16 v234, v215, v197, v234 op_sel:[1,1,0] op_sel_hi:[0,1,1] neg_lo:[1,0,0]
	v_pk_mul_f16 v235, v216, v197 op_sel_hi:[1,0]
	v_pk_fma_f16 v235, v216, v197, v235 op_sel:[1,1,0] op_sel_hi:[0,1,1] neg_lo:[1,0,0]
	ds_write_addtid_b32 v233 offset:57792
	ds_write_addtid_b32 v234 offset:59856
	v_pk_mul_f16 v218, v217, v190 op_sel_hi:[1,0]
	v_pk_fma_f16 v218, v217, v190, v218 op_sel:[1,1,0] op_sel_hi:[0,1,1] neg_hi:[1,0,0]
	s_waitcnt lgkmcnt(14)
	v_mfma_f32_32x32x16_f16 v[64:79], v[108:111], v[140:143], v[64:79]
	v_pk_mul_f16 v219, v217, v191 op_sel_hi:[1,0]
	v_pk_fma_f16 v219, v217, v191, v219 op_sel:[1,1,0] op_sel_hi:[0,1,1] neg_hi:[1,0,0]
	ds_write_addtid_b32 v235 offset:61920
	s_mov_b32 m0, s49
	s_nop 0
	ds_write_addtid_b32 v217 offset:0
	ds_write_addtid_b32 v218 offset:2064
	ds_read_b128 v[112:115], v30 offset:43344
	ds_read_b128 v[116:119], v30 offset:43376
	v_pk_mul_f16 v220, v218, v191 op_sel_hi:[1,0]
	v_pk_fma_f16 v220, v218, v191, v220 op_sel:[1,1,0] op_sel_hi:[0,1,1] neg_hi:[1,0,0]
	v_pk_mul_f16 v221, v217, v192 op_sel_hi:[1,0]
	v_pk_fma_f16 v221, v217, v192, v221 op_sel:[1,1,0] op_sel_hi:[0,1,1] neg_hi:[1,0,0]
	ds_write_addtid_b32 v219 offset:4128
	ds_write_addtid_b32 v220 offset:6192
	v_pk_mul_f16 v222, v218, v192 op_sel_hi:[1,0]
	v_pk_fma_f16 v222, v218, v192, v222 op_sel:[1,1,0] op_sel_hi:[0,1,1] neg_hi:[1,0,0]
	v_pk_mul_f16 v223, v219, v192 op_sel_hi:[1,0]
	v_pk_fma_f16 v223, v219, v192, v223 op_sel:[1,1,0] op_sel_hi:[0,1,1] neg_hi:[1,0,0]
	ds_write_addtid_b32 v221 offset:8256
	ds_write_addtid_b32 v222 offset:10320
	v_pk_mul_f16 v224, v220, v192 op_sel_hi:[1,0]
	v_pk_fma_f16 v224, v220, v192, v224 op_sel:[1,1,0] op_sel_hi:[0,1,1] neg_hi:[1,0,0]
	v_pk_mul_f16 v225, v217, v198 op_sel_hi:[1,0]
	v_pk_fma_f16 v225, v217, v198, v225 op_sel:[1,1,0] op_sel_hi:[0,1,1] neg_hi:[1,0,0]
	ds_write_addtid_b32 v223 offset:12384
	ds_read_b128 v[120:123], v30 offset:43408
	ds_read_b128 v[124:127], v30 offset:43440
	ds_write_addtid_b32 v224 offset:14448
	v_pk_mul_f16 v226, v217, v199 op_sel_hi:[1,0]
	v_pk_fma_f16 v226, v217, v199, v226 op_sel:[1,1,0] op_sel_hi:[0,1,1] neg_hi:[1,0,0]
	v_pk_mul_f16 v227, v225, v199 op_sel_hi:[1,0]
	v_pk_fma_f16 v227, v225, v199, v227 op_sel:[1,1,0] op_sel_hi:[0,1,1] neg_hi:[1,0,0]
	ds_write_addtid_b32 v225 offset:16512
	ds_write_addtid_b32 v226 offset:24768
	v_pk_mul_f16 v228, v226, v199 op_sel_hi:[1,0]
	v_pk_fma_f16 v228, v226, v199, v228 op_sel:[1,1,0] op_sel_hi:[0,1,1] neg_hi:[1,0,0]
	v_pk_mul_f16 v229, v225, v191 op_sel_hi:[1,0]
	v_pk_fma_f16 v229, v225, v191, v229 op_sel:[1,1,0] op_sel_hi:[0,1,1] neg_hi:[1,0,0]
	ds_write_addtid_b32 v227 offset:28896
	ds_write_addtid_b32 v228 offset:33024
	v_pk_mul_f16 v204, v227, v199 op_sel_hi:[1,0]
	v_pk_fma_f16 v204, v227, v199, v204 op_sel:[1,1,0] op_sel_hi:[0,1,1] neg_hi:[1,0,0]
	v_pk_mul_f16 v206, v228, v199 op_sel_hi:[1,0]
	v_pk_fma_f16 v206, v228, v199, v206 op_sel:[1,1,0] op_sel_hi:[0,1,1] neg_hi:[1,0,0]
	ds_read_b128 v[128:131], v30 offset:43472
	ds_read_b128 v[132:135], v30 offset:43504
	ds_write_addtid_b32 v229 offset:20640
	ds_write_addtid_b32 v204 offset:35088
	v_pk_mul_f16 v236, v225, v192 op_sel_hi:[1,0]
	v_pk_fma_f16 v236, v225, v192, v236 op_sel:[1,1,0] op_sel_hi:[0,1,1] neg_hi:[1,0,0]
	v_pk_mul_f16 v237, v226, v192 op_sel_hi:[1,0]
	v_pk_fma_f16 v237, v226, v192, v237 op_sel:[1,1,0] op_sel_hi:[0,1,1] neg_hi:[1,0,0]
	ds_write_addtid_b32 v206 offset:37152
	ds_write_addtid_b32 v236 offset:18576
	v_pk_mul_f16 v238, v227, v192 op_sel_hi:[1,0]
	v_pk_fma_f16 v238, v227, v192, v238 op_sel:[1,1,0] op_sel_hi:[0,1,1] neg_hi:[1,0,0]
	v_pk_mul_f16 v239, v229, v192 op_sel_hi:[1,0]
	v_pk_fma_f16 v239, v229, v192, v239 op_sel:[1,1,0] op_sel_hi:[0,1,1] neg_hi:[1,0,0]
	ds_write_addtid_b32 v237 offset:26832
	ds_write_addtid_b32 v238 offset:30960
	v_pk_mul_f16 v230, v204, v199 op_sel_hi:[1,0]
	v_pk_fma_f16 v230, v204, v199, v230 op_sel:[1,1,0] op_sel_hi:[0,1,1] neg_hi:[1,0,0]
	ds_read_b128 v[136:139], v30 offset:43536
	ds_read_b128 v[140:143], v30 offset:43568
	v_pk_mul_f16 v231, v206, v199 op_sel_hi:[1,0]
	v_pk_fma_f16 v231, v206, v199, v231 op_sel:[1,1,0] op_sel_hi:[0,1,1] neg_hi:[1,0,0]
	ds_write_addtid_b32 v239 offset:22704
	ds_write_addtid_b32 v230 offset:39216
	ds_write_addtid_b32 v231 offset:41280
	s_setprio 1
	ds_read_b128 v[80:83], v30 offset:0
	ds_read_b128 v[84:87], v30 offset:32
	ds_read_b128 v[88:91], v30 offset:64
	ds_read_b128 v[92:95], v30 offset:96
	ds_read_b128 v[96:99], v30 offset:128
	ds_read_b128 v[100:103], v30 offset:160
	ds_read_b128 v[104:107], v30 offset:192
	ds_read_b128 v[108:111], v30 offset:224
	s_waitcnt vmcnt(0)
	s_mul_i32 s42, s33, 2432
	s_mul_i32 s43, s33, 2176
	v_add_u32 v32, s53, v32
	v_add_u32 v33, s53, v33
	v_add_u32 v34, s53, v34
	v_add_u32 v36, s42, v36
	v_add_u32 v37, s43, v37
	v_add_u32 v38, s43, v38
	v_add_u32 v39, s43, v39
	v_add_u32 v40, s43, v40
	s_waitcnt lgkmcnt(0)
	s_barrier
	v_mfma_f32_32x32x16_f16 v[64:79], v[80:83], v[112:115], v[64:79]
	v_mfma_f32_32x32x16_f16 v[64:79], v[84:87], v[116:119], v[64:79]
	v_mfma_f32_32x32x16_f16 v[64:79], v[88:91], v[120:123], v[64:79]
	v_mfma_f32_32x32x16_f16 v[64:79], v[92:95], v[124:127], v[64:79]
	v_mfma_f32_32x32x16_f16 v[64:79], v[96:99], v[128:131], v[64:79]
	v_mfma_f32_32x32x16_f16 v[64:79], v[100:103], v[132:135], v[64:79]
	v_mfma_f32_32x32x16_f16 v[64:79], v[104:107], v[136:139], v[64:79]
	v_mfma_f32_32x32x16_f16 v[64:79], v[108:111], v[140:143], v[64:79]
	s_nop 11
	v_mul_f32 v68, s48, v68
	v_mul_f32 v69, s48, v69
	v_mul_f32 v70, s48, v70
	v_mul_f32 v71, s48, v71
	v_mul_f32 v72, s48, v72
	v_mul_f32 v73, s48, v73
	v_mul_f32 v74, s48, v74
	v_mul_f32 v75, s48, v75
	v_mul_f32 v65, s48, v65
	v_mul_f32 v66, s48, v66
	v_mul_f32 v67, s48, v67
	v_fma_f32 v64, v64, s48, v251
	v_cvt_pk_bf16_f32 v202, v68, v69
	v_cvt_pk_bf16_f32 v203, v70, v71
	v_cvt_pk_bf16_f32 v204, v72, v73
	v_cvt_pk_bf16_f32 v205, v74, v75
	v_cvt_pk_bf16_f32 v200, v67, v66
	v_cvt_pk_bf16_f32 v201, v65, v64
	v_readlane_b32 s42, v202, 32
	v_readlane_b32 s43, v204, 0
	v_mov_b32_dpp v208, v202 quad_perm:[0,0,0,0] row_mask:0xf bank_mask:0xf
	v_mov_b32_dpp v209, v203 quad_perm:[0,0,0,0] row_mask:0xf bank_mask:0xf
	v_mov_b32_dpp v210, v204 quad_perm:[1,0,3,2] row_mask:0xf bank_mask:0xf
	v_mov_b32_dpp v211, v205 quad_perm:[1,0,3,2] row_mask:0xf bank_mask:0xf
	s_and_b32 s42, s42, 0xffff
	v_and_b32 v206, v250, v202
	v_alignbit_b32 v207, v201, v200, 16
	v_perm_b32 v212, v209, v208, v253
	v_perm_b32 v213, v209, v208, v249
	v_writelane_b32 v204, s43, 33
	v_writelane_b32 v206, s42, 0
	v_writelane_b32 v203, s42, 0
	v_perm_b32 v211, v211, v205, v254
	v_perm_b32 v210, v210, v204, v254
	s_add_i32 m0, s51, -64800
	s_mov_b64 exec, 15
	ds_write_addtid_b32 v212 offset:65456
	ds_write_addtid_b32 v213 offset:64848
	s_mov_b64 exec, -1
	ds_write_b64 v244, v[200:201]
	ds_write_b16 v244, v200 offset:606
	ds_write_b32 v244, v207 offset:608
	ds_write_b16_d16_hi v244, v201 offset:612
	ds_write_b32 v245, v206
	ds_write_b32 v246, v203
	s_mov_b32 exec_hi, 3
	ds_write_b32 v247, v210 offset:752
	s_mov_b32 exec_hi, 0
	ds_write_b32 v247, v211 offset:880
	s_mov_b64 exec, -1
	s_waitcnt lgkmcnt(0)
	s_barrier
	s_lshl_b32 s42, s33, 8
	s_add_i32 s42, s42, 20096
	s_mov_b32 m0, s42
	s_setprio 0
	ds_read_b128 v[0:3], v32 offset:192
	ds_read2_b32 v[4:5], v36 offset0:128 offset1:129
	ds_read2_b32 v[6:7], v36 offset0:130 offset1:131
	ds_read_b128 v[8:11], v32 offset:448
	ds_read_b128 v[12:15], v32 offset:128
	ds_read2_b32 v[16:17], v36 offset0:112 offset1:113
	ds_read2_b32 v[18:19], v36 offset0:114 offset1:115
	ds_read_b128 v[20:23], v32 offset:384
	ds_read_b128 v[24:27], v32 offset:64
	ds_read2_b32 v[28:29], v36 offset0:96 offset1:97
	ds_read2_b32 v[30:31], v36 offset0:98 offset1:99
	ds_read2_b32 v[52:53], v36 offset0:80 offset1:81
	ds_read2_b32 v[54:55], v36 offset0:82 offset1:83
	ds_read2_b32 v[56:57], v36 offset0:64 offset1:65
	ds_read2_b32 v[58:59], v36 offset0:66 offset1:67
	ds_read2_b32 v[60:61], v36 offset0:48 offset1:49
	ds_read2_b32 v[62:63], v36 offset0:50 offset1:51
	ds_read2_b32 v[64:65], v36 offset0:32 offset1:33
	ds_read2_b32 v[66:67], v36 offset0:34 offset1:35
	ds_read2_b32 v[68:69], v36 offset0:16 offset1:17
	ds_read2_b32 v[70:71], v36 offset0:18 offset1:19
	s_waitcnt lgkmcnt(14)
	v_mfma_f32_16x16x32_bf16 v[72:75], v[0:3], v[4:7], 0
	s_waitcnt lgkmcnt(12)
	ds_read_b128 v[76:79], v32 offset:320
	ds_read_b128 v[80:83], v32 offset:0
	v_mfma_f32_16x16x32_bf16 v[84:87], v[8:11], v[4:7], 0
	s_waitcnt lgkmcnt(13)
	ds_read_b128 v[88:91], v32 offset:256
	v_mfma_f32_16x16x32_bf16 v[72:75], v[12:15], v[16:19], v[72:75]
	s_waitcnt lgkmcnt(13)
	ds_read_b128 v[92:95], v32 offset:704
	v_mfma_f32_16x16x32_bf16 v[84:87], v[20:23], v[16:19], v[84:87]
	s_waitcnt lgkmcnt(12)
	ds_read_b128 v[96:99], v35 offset:0
	v_mfma_f32_16x16x32_bf16 v[72:75], v[24:27], v[28:31], v[72:75]
	s_waitcnt lgkmcnt(3)
	ds_read_b128 v[100:103], v32 offset:640
	v_mfma_f32_16x16x32_bf16 v[84:87], v[76:79], v[28:31], v[84:87]
	ds_read_b128 v[104:107], v32 offset:576
	v_mfma_f32_16x16x32_bf16 v[72:75], v[80:83], v[52:55], v[72:75]
	s_waitcnt lgkmcnt(3)
	ds_read_b128 v[108:111], v32 offset:512
	v_mfma_f32_16x16x32_bf16 v[84:87], v[88:91], v[52:55], v[84:87]
	ds_read_b128 v[112:115], v32 offset:960
	v_mfma_f32_16x16x32_bf16 v[116:119], v[92:95], v[4:7], 0
	ds_read_b128 v[120:123], v32 offset:896
	v_mfma_f32_16x16x32_bf16 v[84:87], v[0:3], v[56:59], v[84:87]
	s_waitcnt lgkmcnt(3)
	ds_read_b128 v[124:127], v32 offset:832
	v_mfma_f32_16x16x32_bf16 v[116:119], v[100:103], v[16:19], v[116:119]
	ds_read_b128 v[128:131], v32 offset:768
	v_mov_b32_dpp v132, v72 quad_perm:[0,1,2,3] row_mask:0xf bank_mask:0x3
	v_mfma_f32_16x16x32_bf16 v[84:87], v[12:15], v[60:63], v[84:87]
	ds_read2_b32 v[12:13], v36 offset0:0 offset1:1
	v_mov_b32_dpp v133, v72 row_ror:8 row_mask:0xf bank_mask:0x3
	v_mfma_f32_16x16x32_bf16 v[116:119], v[104:107], v[28:31], v[116:119]
	ds_read2_b32 v[14:15], v36 offset0:2 offset1:3
	v_mov_b32_dpp v134, v74 quad_perm:[0,1,2,3] row_mask:0xf bank_mask:0x3
	v_mfma_f32_16x16x32_bf16 v[84:87], v[24:27], v[64:67], v[84:87]
	s_waitcnt lgkmcnt(5)
	ds_read2_b32 v[24:25], v37 offset0:0 offset1:1
	v_mov_b32_dpp v135, v74 row_ror:8 row_mask:0xf bank_mask:0x3
	v_mfma_f32_16x16x32_bf16 v[116:119], v[108:111], v[52:55], v[116:119]
	ds_read_b64_tr_b16 v[136:137], v33 offset:8232
	v_mov_b32_dpp v132, v73 row_ror:8 row_mask:0xf bank_mask:0xc
	v_mfma_f32_16x16x32_bf16 v[84:87], v[80:83], v[68:71], v[84:87]
	ds_read2_b32 v[26:27], v37 offset0:48 offset1:49
	v_mov_b32_dpp v133, v73 quad_perm:[0,1,2,3] row_mask:0xf bank_mask:0xc
	v_mfma_f32_16x16x32_bf16 v[116:119], v[8:11], v[56:59], v[116:119]
	ds_read_b64_tr_b16 v[138:139], v33 offset:4480
	v_mov_b32_dpp v134, v75 row_ror:8 row_mask:0xf bank_mask:0xc
	v_mfma_f32_16x16x32_bf16 v[80:83], v[112:115], v[4:7], 0
	ds_read2_b32 v[140:141], v37 offset0:8 offset1:9
	v_mov_b32_dpp v135, v75 quad_perm:[0,1,2,3] row_mask:0xf bank_mask:0xc
	v_mfma_f32_16x16x32_bf16 v[116:119], v[20:23], v[60:63], v[116:119]
	s_waitcnt lgkmcnt(8)
	ds_read_b64_tr_b16 v[20:21], v33 offset:8104
	v_sub_f32 v132, v132, v133
	v_mfma_f32_16x16x32_bf16 v[80:83], v[120:123], v[16:19], v[80:83]
	ds_read2_b32 v[142:143], v37 offset0:56 offset1:57
	v_sub_f32 v134, v134, v135
	v_mfma_f32_16x16x32_bf16 v[116:119], v[76:79], v[64:67], v[116:119]
	ds_read_b64_tr_b16 v[22:23], v33 offset:4352
	v_mov_b32_dpp v72, v84 quad_perm:[0,1,2,3] row_mask:0xf bank_mask:0x3
	v_mfma_f32_16x16x32_bf16 v[80:83], v[124:127], v[28:31], v[80:83]
	ds_read_b128 v[76:79], v32 offset:1216
	v_fmac_f32 v133, v96, v132
	v_mfma_f32_16x16x32_bf16 v[116:119], v[88:91], v[68:71], v[116:119]
	s_waitcnt lgkmcnt(5)
	ds_read_b128 v[88:91], v35 offset:1024
	v_mov_b32_dpp v73, v84 row_ror:8 row_mask:0xf bank_mask:0x3
	v_mfma_f32_16x16x32_bf16 v[80:83], v[128:131], v[52:55], v[80:83]
	ds_read_b128 v[144:147], v32 offset:1152
	v_fmac_f32 v135, v97, v134
	v_mfma_f32_16x16x32_bf16 v[116:119], v[0:3], v[12:15], v[116:119]
	ds_read_b64_tr_b16 v[0:1], v33 offset:8488
	v_mov_b32_dpp v74, v86 quad_perm:[0,1,2,3] row_mask:0xf bank_mask:0x3
	v_mfma_f32_16x16x32_bf16 v[80:83], v[92:95], v[56:59], v[80:83]
	ds_read_b64_tr_b16 v[2:3], v33 offset:4736
	v_cvt_pk_bf16_f32 v132, v133, v135
	v_mfma_f32_16x16x32_bf16 v[148:151], v[24:27], v[136:139], 0
	ds_read_b128 v[152:155], v32 offset:1088
	v_mov_b32_dpp v75, v86 row_ror:8 row_mask:0xf bank_mask:0x3
	v_mfma_f32_16x16x32_bf16 v[80:83], v[100:103], v[60:63], v[80:83]
	s_waitcnt lgkmcnt(5)
	ds_read_b64_tr_b16 v[100:101], v33 offset:8360
	v_mov_b32_dpp v72, v85 row_ror:8 row_mask:0xf bank_mask:0xc
	v_mfma_f32_16x16x32_bf16 v[148:151], v[140:143], v[20:23], v[148:151]
	ds_read_b64_tr_b16 v[102:103], v33 offset:4608
	v_mov_b32_dpp v73, v85 quad_perm:[0,1,2,3] row_mask:0xf bank_mask:0xc
	v_mfma_f32_16x16x32_bf16 v[80:83], v[104:107], v[64:67], v[80:83]
	ds_read_b128 v[104:107], v32 offset:1024
	v_mov_b32_dpp v74, v87 row_ror:8 row_mask:0xf bank_mask:0xc
	v_mfma_f32_16x16x32_bf16 v[80:83], v[108:111], v[68:71], v[80:83]
	ds_write_addtid_b32 v132 offset:49152
	v_mov_b32_dpp v75, v87 quad_perm:[0,1,2,3] row_mask:0xf bank_mask:0xc
	v_mfma_f32_16x16x32_bf16 v[84:87], v[76:79], v[4:7], 0
	ds_read2_b32 v[108:109], v37 offset0:16 offset1:17
	v_sub_f32 v72, v72, v73
	v_mfma_f32_16x16x32_bf16 v[80:83], v[8:11], v[12:15], v[80:83]
	s_waitcnt lgkmcnt(3)
	ds_read2_b32 v[110:111], v37 offset0:64 offset1:65
	v_sub_f32 v74, v74, v75
	v_mfma_f32_16x16x32_bf16 v[84:87], v[144:147], v[16:19], v[84:87]
	ds_read2_b32 v[8:9], v37 offset0:24 offset1:25
	v_fmac_f32 v73, v98, v72
	v_mfma_f32_16x16x32_bf16 v[132:135], v[24:27], v[0:3], 0
	ds_read2_b32 v[10:11], v37 offset0:72 offset1:73
	v_fmac_f32 v75, v99, v74
	v_mfma_f32_16x16x32_bf16 v[84:87], v[152:155], v[28:31], v[84:87]
	ds_read_b64_tr_b16 v[156:157], v33 offset:8744
	v_cvt_pk_bf16_f32 v72, v73, v75
	v_mfma_f32_16x16x32_bf16 v[132:135], v[140:143], v[100:103], v[132:135]
	s_waitcnt lgkmcnt(3)
	ds_read2_b32 v[164:165], v38 offset0:0 offset1:1
	v_add_f32_dpp v148, v116, v148 quad_perm:[0,1,2,3] row_mask:0xf bank_mask:0x3
	v_mfma_f32_16x16x32_bf16 v[84:87], v[104:107], v[52:55], v[84:87]
	ds_read_b64_tr_b16 v[158:159], v33 offset:4992
	v_add_f32_dpp v149, v116, v149 row_ror:8 row_mask:0xf bank_mask:0x3
	v_mfma_f32_16x16x32_bf16 v[132:135], v[108:111], v[136:139], v[132:135]
	ds_read2_b32 v[166:167], v38 offset0:2 offset1:3
	v_add_f32_dpp v150, v118, v150 quad_perm:[0,1,2,3] row_mask:0xf bank_mask:0x3
	v_mfma_f32_16x16x32_bf16 v[84:87], v[112:115], v[56:59], v[84:87]
	s_waitcnt lgkmcnt(3)
	ds_read_b32 v168, v38 offset:16
	ds_read_b64_tr_b16 v[172:173], v33 offset:8616
	v_mfma_f32_16x16x32_bf16 v[132:135], v[8:11], v[20:23], v[132:135]
	ds_read_b64_tr_b16 v[174:175], v33 offset:4864
	ds_write_addtid_b32 v72 offset:50176
	v_mfma_f32_16x16x32_bf16 v[84:87], v[120:123], v[60:63], v[84:87]
	ds_read_b128 v[20:23], v32 offset:1472
	v_add_f32_dpp v151, v118, v151 row_ror:8 row_mask:0xf bank_mask:0x3
	v_mfma_f32_16x16x32_bf16 v[84:87], v[124:127], v[64:67], v[84:87]
	s_waitcnt lgkmcnt(1)
	s_barrier
	v_add_f32_dpp v148, v117, v148 row_ror:8 row_mask:0xf bank_mask:0xc
	v_add_f32_dpp v149, v117, v149 quad_perm:[0,1,2,3] row_mask:0xf bank_mask:0xc
	v_mfma_f32_16x16x32_bf16 v[72:75], v[24:27], v[156:159], 0
	v_add_f32_dpp v150, v119, v150 row_ror:8 row_mask:0xf bank_mask:0xc
	v_add_f32_dpp v151, v119, v151 quad_perm:[0,1,2,3] row_mask:0xf bank_mask:0xc
	v_mfma_f32_16x16x32_bf16 v[84:87], v[128:131], v[68:71], v[84:87]
	ds_read_b128 v[96:99], v32 offset:1408
	v_sub_f32 v148, v148, v149
	v_mfma_f32_16x16x32_bf16 v[72:75], v[140:143], v[172:175], v[72:75]
	ds_read2_b32 v[116:117], v37 offset0:32 offset1:33
	v_perm_b32 v160, v165, v164, s59
	v_mfma_f32_16x16x32_bf16 v[84:87], v[92:95], v[12:15], v[84:87]
	ds_read2_b32 v[118:119], v37 offset0:80 offset1:81
	ds_read_b128 v[92:95], v32 offset:1344
	v_mfma_f32_16x16x32_bf16 v[72:75], v[108:111], v[0:3], v[72:75]
	s_waitcnt lgkmcnt(4)
	ds_read_b64_tr_b16 v[120:121], v34 offset:8128
	v_perm_b32 v161, v167, v166, s59
	v_sub_f32 v150, v150, v151
	v_mfma_f32_16x16x32_bf16 v[124:127], v[20:23], v[4:7], 0
	ds_read_b64_tr_b16 v[122:123], v34 offset:11864
	ds_read_b128 v[128:131], v32 offset:1280
	v_perm_b32 v162, v166, v165, s58
	v_mfma_f32_16x16x32_bf16 v[72:75], v[8:11], v[100:103], v[72:75]
	s_waitcnt lgkmcnt(5)
	ds_read_b64_tr_b16 v[100:101], v33 offset:9000
	v_fmac_f32 v149, v88, v148
	v_perm_b32 v163, v168, v167, s58
	v_mfma_f32_16x16x32_bf16 v[124:127], v[96:99], v[16:19], v[124:127]
	s_waitcnt lgkmcnt(4)
	ds_read_b64_tr_b16 v[102:103], v33 offset:5248
	ds_read_b128 v[164:167], v35 offset:2048
	v_fmac_f32 v151, v89, v150
	v_mfma_f32_16x16x32_bf16 v[72:75], v[116:119], v[136:139], v[72:75]
	ds_read_b64_tr_b16 v[136:137], v33 offset:8872
	ds_read2_b32 v[176:177], v38 offset0:16 offset1:17
	v_cvt_pk_bf16_f32 v148, v149, v151
	v_mfma_f32_16x16x32_bf16 v[124:127], v[92:95], v[28:31], v[124:127]
	s_waitcnt lgkmcnt(5)
	ds_read_b64_tr_b16 v[138:139], v33 offset:5120
	ds_read2_b32 v[178:179], v38 offset0:18 offset1:19
	v_add_f32_dpp v132, v80, v132 quad_perm:[0,1,2,3] row_mask:0xf bank_mask:0x3
	v_mfma_f32_16x16x32_bf16 v[72:75], v[160:163], v[120:123], v[72:75]
	ds_read_b32 v180, v38 offset:80
	ds_write_addtid_b32 v148 offset:51200
	v_add_f32_dpp v133, v80, v133 row_ror:8 row_mask:0xf bank_mask:0x3
	v_mfma_f32_16x16x32_bf16 v[124:127], v[128:131], v[52:55], v[124:127]
	s_waitcnt lgkmcnt(7)
	v_add_f32_dpp v134, v82, v134 quad_perm:[0,1,2,3] row_mask:0xf bank_mask:0x3
	v_add_f32_dpp v135, v82, v135 row_ror:8 row_mask:0xf bank_mask:0x3
	v_add_f32_dpp v132, v81, v132 row_ror:8 row_mask:0xf bank_mask:0xc
	v_mfma_f32_16x16x32_bf16 v[148:151], v[24:27], v[100:103], 0
	ds_read_b64_tr_b16 v[184:185], v34 offset:8384
	v_add_f32_dpp v133, v81, v133 quad_perm:[0,1,2,3] row_mask:0xf bank_mask:0xc
	v_mfma_f32_16x16x32_bf16 v[124:127], v[76:79], v[56:59], v[124:127]
	s_waitcnt lgkmcnt(4)
	ds_read_b64_tr_b16 v[186:187], v34 offset:12120
	v_add_f32_dpp v134, v83, v134 row_ror:8 row_mask:0xf bank_mask:0xc
	v_mfma_f32_16x16x32_bf16 v[148:151], v[140:143], v[136:139], v[148:151]
	ds_read_b128 v[188:191], v32 offset:1728
	v_add_f32_dpp v135, v83, v135 quad_perm:[0,1,2,3] row_mask:0xf bank_mask:0xc
	v_mfma_f32_16x16x32_bf16 v[124:127], v[144:147], v[60:63], v[124:127]
	ds_read_b128 v[80:83], v32 offset:1664
	v_sub_f32 v132, v132, v133
	v_mfma_f32_16x16x32_bf16 v[148:151], v[108:111], v[156:159], v[148:151]
	ds_read_b64_tr_b16 v[144:145], v33 offset:9256
	v_sub_f32 v134, v134, v135
	v_mfma_f32_16x16x32_bf16 v[124:127], v[152:155], v[64:67], v[124:127]
	ds_read_b64_tr_b16 v[146:147], v33 offset:5504
	v_perm_b32 v168, v177, v176, s59
	v_fmac_f32 v133, v90, v132
	v_mfma_f32_16x16x32_bf16 v[148:151], v[8:11], v[172:175], v[148:151]
	s_waitcnt lgkmcnt(7)
	ds_read_b128 v[152:155], v32 offset:1600
	v_perm_b32 v169, v179, v178, s59
	v_fmac_f32 v135, v91, v134
	v_mfma_f32_16x16x32_bf16 v[124:127], v[104:107], v[68:71], v[124:127]
	ds_read_b64_tr_b16 v[104:105], v33 offset:9128
	v_perm_b32 v170, v178, v177, s58
	v_cvt_pk_bf16_f32 v132, v133, v135
	v_mfma_f32_16x16x32_bf16 v[148:151], v[116:119], v[0:3], v[148:151]
	ds_write_addtid_b32 v132 offset:52224
	v_perm_b32 v171, v180, v179, s58
	v_add_f32_dpp v72, v84, v72 quad_perm:[0,1,2,3] row_mask:0xf bank_mask:0x3
	v_mfma_f32_16x16x32_bf16 v[124:127], v[112:115], v[12:15], v[124:127]
	s_waitcnt lgkmcnt(5)
	ds_read_b64_tr_b16 v[106:107], v33 offset:5376
	v_add_f32_dpp v73, v84, v73 row_ror:8 row_mask:0xf bank_mask:0x3
	v_mfma_f32_16x16x32_bf16 v[148:151], v[160:163], v[184:187], v[148:151]
	s_waitcnt lgkmcnt(1)
	s_barrier
	v_add_f32_dpp v74, v86, v74 quad_perm:[0,1,2,3] row_mask:0xf bank_mask:0x3
	v_add_f32_dpp v75, v86, v75 row_ror:8 row_mask:0xf bank_mask:0x3
	v_mfma_f32_16x16x32_bf16 v[88:91], v[188:191], v[4:7], 0
	v_add_f32_dpp v72, v85, v72 row_ror:8 row_mask:0xf bank_mask:0xc
	v_add_f32_dpp v73, v85, v73 quad_perm:[0,1,2,3] row_mask:0xf bank_mask:0xc
	v_mfma_f32_16x16x32_bf16 v[148:151], v[168:171], v[120:123], v[148:151]
	ds_read_b128 v[112:115], v32 offset:1536
	ds_read2_b32 v[172:173], v39 offset0:0 offset1:1
	v_mfma_f32_16x16x32_bf16 v[88:91], v[80:83], v[16:19], v[88:91]
	ds_read_b32 v174, v39 offset:8
	ds_read2_b32 v[192:193], v38 offset0:32 offset1:33
	v_mfma_f32_16x16x32_bf16 v[180:183], v[24:27], v[144:147], 0
	ds_read2_b32 v[194:195], v38 offset0:34 offset1:35
	ds_read_b32 v196, v38 offset:144
	v_add_f32_dpp v74, v87, v74 row_ror:8 row_mask:0xf bank_mask:0xc
	v_mfma_f32_16x16x32_bf16 v[88:91], v[152:155], v[28:31], v[88:91]
	s_waitcnt lgkmcnt(6)
	v_add_f32_dpp v75, v87, v75 quad_perm:[0,1,2,3] row_mask:0xf bank_mask:0xc
	v_sub_f32 v72, v72, v73
	v_mfma_f32_16x16x32_bf16 v[180:183], v[140:143], v[104:107], v[180:183]
	s_waitcnt lgkmcnt(5)
	v_sub_f32 v74, v74, v75
	v_fmac_f32 v73, v164, v72
	v_mfma_f32_16x16x32_bf16 v[88:91], v[112:115], v[52:55], v[88:91]
	v_fmac_f32 v75, v165, v74
	v_cvt_pk_bf16_f32 v72, v73, v75
	v_mfma_f32_16x16x32_bf16 v[180:183], v[108:111], v[100:103], v[180:183]
	ds_read2_b32 v[198:199], v39 offset0:8 offset1:9
	ds_read_b32 v200, v39 offset:40
	v_mfma_f32_16x16x32_bf16 v[88:91], v[20:23], v[56:59], v[88:91]
	ds_read_b64_tr_b16 v[204:205], v34 offset:8640
	ds_read_b64_tr_b16 v[206:207], v34 offset:12376
	v_mfma_f32_16x16x32_bf16 v[180:183], v[8:11], v[136:139], v[180:183]
	s_waitcnt lgkmcnt(5)
	ds_read_b128 v[136:139], v32 offset:1984
	v_perm_b32 v176, v193, v192, s59
	v_mfma_f32_16x16x32_bf16 v[88:91], v[96:99], v[60:63], v[88:91]
	ds_read2_b32 v[208:209], v39 offset0:16 offset1:17
	v_perm_b32 v177, v195, v194, s59
	v_mfma_f32_16x16x32_bf16 v[180:183], v[116:119], v[156:159], v[180:183]
	ds_write_addtid_b32 v72 offset:53248
	v_add_f32_dpp v148, v124, v148 quad_perm:[0,1,2,3] row_mask:0xf bank_mask:0x3
	v_mfma_f32_16x16x32_bf16 v[88:91], v[92:95], v[64:67], v[88:91]
	s_waitcnt lgkmcnt(3)
	ds_read_b32 v210, v39 offset:72
	v_perm_b32 v178, v194, v193, s58
	v_add_f32_dpp v149, v124, v149 row_ror:8 row_mask:0xf bank_mask:0x3
	v_mfma_f32_16x16x32_bf16 v[180:183], v[160:163], v[204:207], v[180:183]
	ds_read_b64_tr_b16 v[72:73], v33 offset:9512
	v_perm_b32 v179, v196, v195, s58
	v_add_f32_dpp v150, v126, v150 quad_perm:[0,1,2,3] row_mask:0xf bank_mask:0x3
	v_mfma_f32_16x16x32_bf16 v[88:91], v[128:131], v[68:71], v[88:91]
	ds_read_b64_tr_b16 v[74:75], v33 offset:5760
	ds_read_b128 v[92:95], v32 offset:1920
	v_add_f32_dpp v151, v126, v151 row_ror:8 row_mask:0xf bank_mask:0x3
	v_mfma_f32_16x16x32_bf16 v[180:183], v[168:171], v[184:187], v[180:183]
	ds_read_b128 v[128:131], v35 offset:3072
	ds_read_b64_tr_b16 v[156:157], v33 offset:9384
	v_add_f32_dpp v148, v125, v148 row_ror:8 row_mask:0xf bank_mask:0xc
	v_mfma_f32_16x16x32_bf16 v[88:91], v[76:79], v[12:15], v[88:91]
	ds_read_b64_tr_b16 v[158:159], v33 offset:5632
	ds_read_b128 v[76:79], v32 offset:1856
	v_add_f32_dpp v149, v125, v149 quad_perm:[0,1,2,3] row_mask:0xf bank_mask:0xc
	v_mfma_f32_16x16x32_bf16 v[180:183], v[176:179], v[120:123], v[180:183]
	s_waitcnt lgkmcnt(6)
	ds_read_b128 v[192:195], v32 offset:1792
	v_add_f32_dpp v150, v127, v150 row_ror:8 row_mask:0xf bank_mask:0xc
	v_mfma_f32_16x16x32_bf16 v[212:215], v[136:139], v[4:7], 0
	s_waitcnt lgkmcnt(5)
	ds_read2_b32 v[220:221], v39 offset0:24 offset1:25
	v_add_f32_dpp v151, v127, v151 quad_perm:[0,1,2,3] row_mask:0xf bank_mask:0xc
	v_mfma_f32_16x16x32_bf16 v[124:127], v[24:27], v[72:75], 0
	ds_read_b32 v222, v39 offset:104
	ds_read2_b32 v[228:229], v38 offset0:48 offset1:49
	v_sub_f32 v148, v148, v149
	v_mfma_f32_16x16x32_bf16 v[212:215], v[92:95], v[16:19], v[212:215]
	s_waitcnt lgkmcnt(4)
	ds_read2_b32 v[230:231], v38 offset0:50 offset1:51
	v_sub_f32 v150, v150, v151
	v_mfma_f32_16x16x32_bf16 v[124:127], v[140:143], v[156:159], v[124:127]
	ds_read_b32 v232, v38 offset:208
	v_fmac_f32 v149, v166, v148
	v_mfma_f32_16x16x32_bf16 v[212:215], v[76:79], v[28:31], v[212:215]
	ds_read_b64_tr_b16 v[236:237], v34 offset:8896
	v_fmac_f32 v151, v167, v150
	v_mfma_f32_16x16x32_bf16 v[124:127], v[108:111], v[144:147], v[124:127]
	s_waitcnt lgkmcnt(3)
	ds_read_b64_tr_b16 v[238:239], v34 offset:12632
	v_cvt_pk_bf16_f32 v148, v149, v151
	v_mfma_f32_16x16x32_bf16 v[212:215], v[192:195], v[52:55], v[212:215]
	ds_write_addtid_b32 v148 offset:54272
	v_add_f32_dpp v180, v88, v180 quad_perm:[0,1,2,3] row_mask:0xf bank_mask:0x3
	v_mfma_f32_16x16x32_bf16 v[124:127], v[8:11], v[104:107], v[124:127]
	v_add_f32_dpp v181, v88, v181 row_ror:8 row_mask:0xf bank_mask:0x3
	v_perm_b32 v132, v173, v172, v43
	v_mfma_f32_16x16x32_bf16 v[212:215], v[188:191], v[56:59], v[212:215]
	s_waitcnt lgkmcnt(0)
	s_barrier
	v_add_f32_dpp v182, v90, v182 quad_perm:[0,1,2,3] row_mask:0xf bank_mask:0x3
	v_perm_b32 v133, v174, v173, v44
	v_mfma_f32_16x16x32_bf16 v[124:127], v[116:119], v[100:103], v[124:127]
	v_perm_b32 v224, v229, v228, s59
	v_perm_b32 v225, v231, v230, s59
	v_mfma_f32_16x16x32_bf16 v[212:215], v[80:83], v[60:63], v[212:215]
	v_perm_b32 v226, v230, v229, s58
	v_perm_b32 v84, v199, v198, v41
	v_mfma_f32_16x16x32_bf16 v[124:127], v[160:163], v[236:239], v[124:127]
	v_add_f32_dpp v183, v90, v183 row_ror:8 row_mask:0xf bank_mask:0x3
	v_perm_b32 v227, v232, v231, s58
	v_mfma_f32_16x16x32_bf16 v[212:215], v[152:155], v[64:67], v[212:215]
	v_perm_b32 v134, v173, v172, v45
	v_perm_b32 v85, v200, v199, v41
	v_mfma_f32_16x16x32_bf16 v[124:127], v[168:171], v[204:207], v[124:127]
	ds_read_b64_tr_b16 v[80:81], v34 offset:120
	ds_read_b64_tr_b16 v[82:83], v34 offset:15096
	v_perm_b32 v96, v209, v208, v41
	v_mfma_f32_16x16x32_bf16 v[212:215], v[112:115], v[68:71], v[212:215]
	v_add_f32_dpp v180, v89, v180 row_ror:8 row_mask:0xf bank_mask:0xc
	v_perm_b32 v135, v174, v173, v46
	v_perm_b32 v86, v199, v198, v42
	v_mfma_f32_16x16x32_bf16 v[124:127], v[176:179], v[184:187], v[124:127]
	ds_read2_b32 v[104:105], v39 offset0:32 offset1:33
	ds_read_b32 v106, v39 offset:136
	v_perm_b32 v97, v210, v209, v41
	v_mfma_f32_16x16x32_bf16 v[212:215], v[20:23], v[12:15], v[212:215]
	ds_read_b64_tr_b16 v[20:21], v34 offset:376
	v_perm_b32 v216, v221, v220, v41
	v_add_f32_dpp v181, v89, v181 quad_perm:[0,1,2,3] row_mask:0xf bank_mask:0xc
	v_mfma_f32_16x16x32_bf16 v[124:127], v[224:227], v[120:123], v[124:127]
	s_waitcnt lgkmcnt(3)
	ds_read_b64_tr_b16 v[22:23], v34 offset:15352
	ds_read2_b32 v[148:149], v39 offset0:40 offset1:41
	v_perm_b32 v87, v200, v199, v42
	v_mfma_f32_16x16x32_bf16 v[152:155], v[132:135], v[80:83], 0
	ds_read_b32 v150, v39 offset:168
	v_perm_b32 v98, v209, v208, v42
	v_perm_b32 v217, v222, v221, v41
	v_mfma_f32_16x16x32_bf16 v[164:167], v[84:87], v[80:83], 0
	ds_read_b64_tr_b16 v[172:173], v34 offset:632
	v_add_f32_dpp v182, v91, v182 row_ror:8 row_mask:0xf bank_mask:0xc
	v_perm_b32 v99, v210, v209, v42
	s_nop 1
	v_mfma_f32_16x16x32_bf16 v[196:199], v[96:99], v[80:83], 0
	ds_read2_b32 v[208:209], v39 offset0:48 offset1:49
	v_perm_b32 v218, v221, v220, v42
	v_add_f32_dpp v183, v91, v183 quad_perm:[0,1,2,3] row_mask:0xf bank_mask:0xc
	v_perm_b32 v219, v222, v221, v42
	s_nop 1
	v_mfma_f32_16x16x32_bf16 v[88:91], v[216:219], v[80:83], 0
	s_waitcnt lgkmcnt(4)
	ds_read_b64_tr_b16 v[174:175], v34 offset:15608
	ds_read_b32 v210, v39 offset:200
	v_sub_f32 v180, v180, v181
	v_mfma_f32_16x16x32_bf16 v[164:167], v[132:135], v[20:23], v[164:167]
	ds_read2_b32 v[228:229], v39 offset0:56 offset1:57
	v_sub_f32 v182, v182, v183
	v_fmac_f32 v181, v128, v180
	v_mfma_f32_16x16x32_bf16 v[196:199], v[84:87], v[20:23], v[196:199]
	ds_read_b32 v230, v39 offset:232
	v_fmac_f32 v183, v129, v182
	v_cvt_pk_bf16_f32 v180, v181, v183
	v_mfma_f32_16x16x32_bf16 v[88:91], v[96:99], v[20:23], v[88:91]
	ds_read_b64_tr_b16 v[232:233], v34 offset:888
	ds_write_addtid_b32 v180 offset:55296
	v_mfma_f32_16x16x32_bf16 v[180:183], v[216:219], v[20:23], 0
	s_waitcnt lgkmcnt(5)
	ds_read_b64_tr_b16 v[234:235], v34 offset:15864
	v_perm_b32 v100, v105, v104, v41
	v_mfma_f32_16x16x32_bf16 v[196:199], v[132:135], v[172:175], v[196:199]
	v_add_f32_dpp v124, v212, v124 quad_perm:[0,1,2,3] row_mask:0xf bank_mask:0x3
	v_perm_b32 v101, v106, v105, v41
	v_mfma_f32_16x16x32_bf16 v[88:91], v[84:87], v[172:175], v[88:91]
	v_perm_b32 v112, v149, v148, v41
	v_add_f32_dpp v125, v212, v125 row_ror:8 row_mask:0xf bank_mask:0x3
	v_mfma_f32_16x16x32_bf16 v[180:183], v[96:99], v[172:175], v[180:183]
	s_barrier
	v_perm_b32 v102, v105, v104, v42
	v_perm_b32 v113, v150, v149, v41
	v_mfma_f32_16x16x32_bf16 v[240:243], v[216:219], v[172:175], 0
	s_waitcnt lgkmcnt(0)
	v_perm_b32 v200, v209, v208, v41
	v_add_f32_dpp v126, v214, v126 quad_perm:[0,1,2,3] row_mask:0xf bank_mask:0x3
	v_mfma_f32_16x16x32_bf16 v[88:91], v[132:135], v[232:235], v[88:91]
	v_perm_b32 v103, v106, v105, v42
	v_perm_b32 v114, v149, v148, v42
	v_perm_b32 v201, v210, v209, v41
	v_mfma_f32_16x16x32_bf16 v[180:183], v[84:87], v[232:235], v[180:183]
	v_perm_b32 v220, v229, v228, v41
	v_add_f32_dpp v127, v214, v127 row_ror:8 row_mask:0xf bank_mask:0x3
	v_perm_b32 v115, v150, v149, v42
	v_mfma_f32_16x16x32_bf16 v[240:243], v[96:99], v[232:235], v[240:243]
	v_perm_b32 v202, v209, v208, v42
	v_perm_b32 v221, v230, v229, v41
	v_add_f32_dpp v124, v213, v124 row_ror:8 row_mask:0xf bank_mask:0xc
	v_mfma_f32_16x16x32_bf16 v[104:107], v[216:219], v[232:235], 0
	v_perm_b32 v203, v210, v209, v42
	v_perm_b32 v222, v229, v228, v42
	v_add_f32_dpp v125, v213, v125 quad_perm:[0,1,2,3] row_mask:0xf bank_mask:0xc
	v_mfma_f32_16x16x32_bf16 v[180:183], v[100:103], v[80:83], v[180:183]
	v_perm_b32 v223, v230, v229, v42
	v_add_f32_dpp v126, v215, v126 row_ror:8 row_mask:0xf bank_mask:0xc
	v_add_f32_dpp v127, v215, v127 quad_perm:[0,1,2,3] row_mask:0xf bank_mask:0xc
	v_mfma_f32_16x16x32_bf16 v[240:243], v[112:115], v[80:83], v[240:243]
	v_sub_f32 v124, v124, v125
	v_sub_f32 v126, v126, v127
	v_mfma_f32_16x16x32_bf16 v[104:107], v[200:203], v[80:83], v[104:107]
	v_fmac_f32 v125, v130, v124
	v_fmac_f32 v127, v131, v126
	v_mfma_f32_16x16x32_bf16 v[148:151], v[220:223], v[80:83], 0
	v_cvt_pk_bf16_f32 v124, v125, v127
	v_mfma_f32_16x16x32_bf16 v[240:243], v[100:103], v[20:23], v[240:243]
	v_mfma_f32_16x16x32_bf16 v[104:107], v[112:115], v[20:23], v[104:107]
	ds_read_b64_tr_b16 v[80:81], v34 offset:1144
	ds_read_b64_tr_b16 v[82:83], v34 offset:16120
	v_mfma_f32_16x16x32_bf16 v[148:151], v[200:203], v[20:23], v[148:151]
	ds_read_b64_tr_b16 v[20:21], v34 offset:1400
	v_mfma_f32_16x16x32_bf16 v[104:107], v[100:103], v[172:175], v[104:107]
	ds_write_addtid_b32 v124 offset:56320
	v_mfma_f32_16x16x32_bf16 v[148:151], v[112:115], v[172:175], v[148:151]
	ds_read_b64_tr_b16 v[22:23], v34 offset:16376
	v_mfma_f32_16x16x32_bf16 v[148:151], v[100:103], v[232:235], v[148:151]
	s_waitcnt lgkmcnt(3)
	ds_read_b64_tr_b16 v[100:101], v34 offset:1656
	v_mfma_f32_16x16x32_bf16 v[180:183], v[132:135], v[80:83], v[180:183]
	ds_read_b64_tr_b16 v[102:103], v34 offset:16632
	v_mfma_f32_16x16x32_bf16 v[240:243], v[84:87], v[80:83], v[240:243]
	s_waitcnt lgkmcnt(3)
	s_barrier
	v_mfma_f32_16x16x32_bf16 v[104:107], v[96:99], v[80:83], v[104:107]
	v_mfma_f32_16x16x32_bf16 v[148:151], v[216:219], v[80:83], v[148:151]
	s_waitcnt lgkmcnt(1)
	v_mfma_f32_16x16x32_bf16 v[240:243], v[132:135], v[20:23], v[240:243]
	ds_read_b64_tr_b16 v[80:81], v34 offset:1912
	ds_read_b64_tr_b16 v[82:83], v34 offset:16888
	v_mfma_f32_16x16x32_bf16 v[104:107], v[84:87], v[20:23], v[104:107]
	ds_read_b128 v[112:115], v32 offset:2240
	ds_read_b64_tr_b16 v[124:125], v33 offset:9768
	v_mfma_f32_16x16x32_bf16 v[148:151], v[96:99], v[20:23], v[148:151]
	s_waitcnt lgkmcnt(4)
	v_mfma_f32_16x16x32_bf16 v[104:107], v[132:135], v[100:103], v[104:107]
	ds_read_b64_tr_b16 v[126:127], v33 offset:6016
	ds_read_b128 v[20:23], v32 offset:2176
	v_mfma_f32_16x16x32_bf16 v[148:151], v[84:87], v[100:103], v[148:151]
	s_waitcnt lgkmcnt(4)
	ds_read_b64_tr_b16 v[84:85], v33 offset:9640
	ds_read_b64_tr_b16 v[86:87], v33 offset:5888
	v_mfma_f32_16x16x32_bf16 v[148:151], v[132:135], v[80:83], v[148:151]
	s_waitcnt lgkmcnt(4)
	ds_read_b128 v[80:83], v32 offset:2112
	v_mfma_f32_16x16x32_bf16 v[96:99], v[112:115], v[4:7], 0
	s_waitcnt lgkmcnt(4)
	v_mfma_f32_16x16x32_bf16 v[152:155], v[24:27], v[124:127], v[152:155]
	s_waitcnt lgkmcnt(3)
	ds_read_b128 v[100:103], v32 offset:2048
	ds_read_b32 v36, v40 offset:668
	v_mfma_f32_16x16x32_bf16 v[96:99], v[20:23], v[16:19], v[96:99]
	s_waitcnt lgkmcnt(3)
	v_mfma_f32_16x16x32_bf16 v[152:155], v[140:143], v[84:87], v[152:155]
	s_waitcnt lgkmcnt(2)
	v_mfma_f32_16x16x32_bf16 v[96:99], v[80:83], v[28:31], v[96:99]
	ds_read2_b32 v[208:209], v38 offset0:64 offset1:65
	ds_read2_b32 v[210:211], v38 offset0:66 offset1:67
	v_mfma_f32_16x16x32_bf16 v[152:155], v[108:111], v[72:75], v[152:155]
	s_waitcnt lgkmcnt(2)
	ds_read_b32 v212, v38 offset:272
	ds_read_b64_tr_b16 v[172:173], v34 offset:9152
	v_mfma_f32_16x16x32_bf16 v[96:99], v[100:103], v[52:55], v[96:99]
	ds_read_b64_tr_b16 v[174:175], v34 offset:12888
	v_mfma_f32_16x16x32_bf16 v[152:155], v[8:11], v[156:159], v[152:155]
	ds_read_b128 v[156:159], v32 offset:192
	v_mfma_f32_16x16x32_bf16 v[96:99], v[136:139], v[56:59], v[96:99]
	ds_read_b128 v[200:203], v32 offset:2496
	v_mfma_f32_16x16x32_bf16 v[152:155], v[116:119], v[144:147], v[152:155]
	v_perm_b32 v36, v36, v36, s58
	v_and_b32 v128, v36, v47
	v_mfma_f32_16x16x32_bf16 v[96:99], v[92:95], v[60:63], v[96:99]
	s_waitcnt lgkmcnt(2)
	v_and_b32 v129, v36, v48
	v_and_b32 v130, v36, v49
	v_mfma_f32_16x16x32_bf16 v[152:155], v[160:163], v[172:175], v[152:155]
	ds_read_b64_tr_b16 v[92:93], v33 offset:10024
	v_perm_b32 v132, v209, v208, s59
	v_mfma_f32_16x16x32_bf16 v[96:99], v[76:79], v[64:67], v[96:99]
	ds_read_b64_tr_b16 v[94:95], v33 offset:6272
	v_perm_b32 v133, v211, v210, s59
	v_mfma_f32_16x16x32_bf16 v[152:155], v[168:171], v[236:239], v[152:155]
	ds_read_b128 v[76:79], v32 offset:2432
	v_and_b32 v131, v36, v50
	v_mfma_f32_16x16x32_bf16 v[96:99], v[192:195], v[68:71], v[96:99]
	ds_read_b128 v[40:43], v35 offset:4096
	v_perm_b32 v134, v210, v209, s58
	v_mfma_f32_16x16x32_bf16 v[152:155], v[176:179], v[204:207], v[152:155]
	ds_read_b64_tr_b16 v[44:45], v33 offset:9896
	v_perm_b32 v135, v212, v211, s58
	v_mfma_f32_16x16x32_bf16 v[96:99], v[188:191], v[12:15], v[96:99]
	ds_read_b64_tr_b16 v[46:47], v33 offset:6144
	v_mfma_f32_16x16x32_bf16 v[152:155], v[224:227], v[184:187], v[152:155]
	s_waitcnt lgkmcnt(4)
	ds_read_b128 v[36:39], v32 offset:2368
	v_mfma_f32_16x16x32_bf16 v[96:99], v[156:159], v[128:131], v[96:99]
	ds_read_b128 v[48:51], v32 offset:2304
	v_mfma_f32_16x16x32_bf16 v[152:155], v[132:135], v[120:123], v[152:155]
	ds_read_b64_tr_b16 v[120:121], v34 offset:9408
	v_mfma_f32_16x16x32_bf16 v[144:147], v[200:203], v[4:7], 0
	ds_read_b64_tr_b16 v[122:123], v34 offset:13144
	v_mfma_f32_16x16x32_bf16 v[164:167], v[24:27], v[92:95], v[164:167]
	s_waitcnt lgkmcnt(3)
	ds_read_b128 v[156:159], v32 offset:448
	v_mfma_f32_16x16x32_bf16 v[144:147], v[76:79], v[16:19], v[144:147]
	ds_read_b128 v[188:191], v32 offset:2752
	v_mfma_f32_16x16x32_bf16 v[164:167], v[140:143], v[44:47], v[164:167]
	ds_read_b64_tr_b16 v[192:193], v33 offset:10280
	v_add_f32_dpp v152, v96, v152 quad_perm:[0,1,2,3] row_mask:0xf bank_mask:0x3
	v_mfma_f32_16x16x32_bf16 v[144:147], v[36:39], v[28:31], v[144:147]
	ds_read_b64_tr_b16 v[194:195], v33 offset:6528
	v_add_f32_dpp v153, v96, v153 row_ror:8 row_mask:0xf bank_mask:0x3
	v_mfma_f32_16x16x32_bf16 v[164:167], v[108:111], v[124:127], v[164:167]
	s_waitcnt lgkmcnt(6)
	ds_read_b128 v[208:211], v32 offset:2688
	v_add_f32_dpp v154, v98, v154 quad_perm:[0,1,2,3] row_mask:0xf bank_mask:0x3
	v_mfma_f32_16x16x32_bf16 v[144:147], v[48:51], v[52:55], v[144:147]
	ds_read_b64_tr_b16 v[212:213], v33 offset:10152
	v_add_f32_dpp v155, v98, v155 row_ror:8 row_mask:0xf bank_mask:0x3
	v_mfma_f32_16x16x32_bf16 v[164:167], v[8:11], v[84:87], v[164:167]
	ds_read_b64_tr_b16 v[214:215], v33 offset:6400
	v_add_f32_dpp v152, v97, v152 row_ror:8 row_mask:0xf bank_mask:0xc
	v_mfma_f32_16x16x32_bf16 v[144:147], v[112:115], v[56:59], v[144:147]
	ds_read_b128 v[84:87], v32 offset:2624
	v_add_f32_dpp v153, v97, v153 quad_perm:[0,1,2,3] row_mask:0xf bank_mask:0xc
	v_mfma_f32_16x16x32_bf16 v[164:167], v[116:119], v[72:75], v[164:167]
	ds_read_b128 v[72:75], v32 offset:2560
	v_add_f32_dpp v154, v99, v154 row_ror:8 row_mask:0xf bank_mask:0xc
	v_mfma_f32_16x16x32_bf16 v[144:147], v[20:23], v[60:63], v[144:147]
	s_waitcnt lgkmcnt(9)
	ds_read_b64_tr_b16 v[20:21], v34 offset:9664
	v_add_f32_dpp v155, v99, v155 quad_perm:[0,1,2,3] row_mask:0xf bank_mask:0xc
	v_mfma_f32_16x16x32_bf16 v[164:167], v[160:163], v[120:123], v[164:167]
	ds_read_b64_tr_b16 v[22:23], v34 offset:13400
	v_sub_f32 v152, v152, v153
	v_mfma_f32_16x16x32_bf16 v[144:147], v[80:83], v[64:67], v[144:147]
	ds_read_b128 v[80:83], v32 offset:704
	v_sub_f32 v154, v154, v155
	v_mfma_f32_16x16x32_bf16 v[164:167], v[168:171], v[172:175], v[164:167]
	ds_read_b128 v[96:99], v32 offset:3008
	v_fmac_f32 v153, v40, v152
	v_mfma_f32_16x16x32_bf16 v[144:147], v[100:103], v[68:71], v[144:147]
	ds_read_b64_tr_b16 v[100:101], v33 offset:10536
	v_fmac_f32 v155, v41, v154
	v_mfma_f32_16x16x32_bf16 v[164:167], v[176:179], v[236:239], v[164:167]
	ds_read_b64_tr_b16 v[102:103], v33 offset:6784
	v_cvt_pk_bf16_f32 v152, v153, v155
	v_mfma_f32_16x16x32_bf16 v[144:147], v[136:139], v[12:15], v[144:147]
	ds_write_addtid_b32 v152 offset:57344
	v_mfma_f32_16x16x32_bf16 v[164:167], v[224:227], v[204:207], v[164:167]
	s_waitcnt lgkmcnt(12)
	ds_read_b128 v[136:139], v32 offset:2944
	v_mfma_f32_16x16x32_bf16 v[144:147], v[156:159], v[128:131], v[144:147]
	s_waitcnt lgkmcnt(1)
	s_barrier
	v_mfma_f32_16x16x32_bf16 v[164:167], v[132:135], v[184:187], v[164:167]
	v_mfma_f32_16x16x32_bf16 v[152:155], v[188:191], v[4:7], 0
	v_mfma_f32_16x16x32_bf16 v[196:199], v[24:27], v[192:195], v[196:199]
	v_mfma_f32_16x16x32_bf16 v[152:155], v[208:211], v[16:19], v[152:155]
	v_mfma_f32_16x16x32_bf16 v[196:199], v[140:143], v[212:215], v[196:199]
	s_nop 3
	v_add_f32_dpp v164, v144, v164 quad_perm:[0,1,2,3] row_mask:0xf bank_mask:0x3
	v_add_f32_dpp v165, v144, v165 row_ror:8 row_mask:0xf bank_mask:0x3
	v_mfma_f32_16x16x32_bf16 v[152:155], v[84:87], v[28:31], v[152:155]
	v_add_f32_dpp v166, v146, v166 quad_perm:[0,1,2,3] row_mask:0xf bank_mask:0x3
	v_add_f32_dpp v167, v146, v167 row_ror:8 row_mask:0xf bank_mask:0x3
	v_mfma_f32_16x16x32_bf16 v[196:199], v[108:111], v[92:95], v[196:199]
	ds_read_b128 v[156:159], v35 offset:5120
	v_add_f32_dpp v164, v145, v164 row_ror:8 row_mask:0xf bank_mask:0xc
	v_mfma_f32_16x16x32_bf16 v[152:155], v[72:75], v[52:55], v[152:155]
	ds_read_b64_tr_b16 v[184:185], v33 offset:10408
	v_add_f32_dpp v165, v145, v165 quad_perm:[0,1,2,3] row_mask:0xf bank_mask:0xc
	v_mfma_f32_16x16x32_bf16 v[196:199], v[8:11], v[44:47], v[196:199]
	ds_read_b64_tr_b16 v[186:187], v33 offset:6656
	v_add_f32_dpp v166, v147, v166 row_ror:8 row_mask:0xf bank_mask:0xc
	v_mfma_f32_16x16x32_bf16 v[152:155], v[200:203], v[56:59], v[152:155]
	ds_read_b128 v[44:47], v32 offset:2880
	v_add_f32_dpp v167, v147, v167 quad_perm:[0,1,2,3] row_mask:0xf bank_mask:0xc
	v_mfma_f32_16x16x32_bf16 v[196:199], v[116:119], v[124:127], v[196:199]
	ds_read_b128 v[124:127], v32 offset:2816
	v_sub_f32 v164, v164, v165
	v_mfma_f32_16x16x32_bf16 v[152:155], v[76:79], v[60:63], v[152:155]
	ds_read_b64_tr_b16 v[76:77], v34 offset:9920
	v_sub_f32 v166, v166, v167
	v_mfma_f32_16x16x32_bf16 v[196:199], v[160:163], v[20:23], v[196:199]
	ds_read_b64_tr_b16 v[78:79], v34 offset:13656
	v_fmac_f32 v165, v42, v164
	v_mfma_f32_16x16x32_bf16 v[152:155], v[36:39], v[64:67], v[152:155]
	ds_read_b128 v[36:39], v32 offset:960
	v_fmac_f32 v167, v43, v166
	v_mfma_f32_16x16x32_bf16 v[196:199], v[168:171], v[120:123], v[196:199]
	ds_read_b64_tr_b16 v[144:145], v33 offset:10792
	v_cvt_pk_bf16_f32 v164, v165, v167
	v_mfma_f32_16x16x32_bf16 v[152:155], v[48:51], v[68:71], v[152:155]
	ds_write_addtid_b32 v164 offset:58368
	v_mfma_f32_16x16x32_bf16 v[196:199], v[176:179], v[172:175], v[196:199]
	ds_read_b64_tr_b16 v[146:147], v33 offset:7040
	v_mfma_f32_16x16x32_bf16 v[152:155], v[112:115], v[12:15], v[152:155]
	ds_read_b64_tr_b16 v[40:41], v33 offset:10664
	v_mfma_f32_16x16x32_bf16 v[196:199], v[224:227], v[236:239], v[196:199]
	ds_read_b64_tr_b16 v[42:43], v33 offset:6912
	v_mfma_f32_16x16x32_bf16 v[152:155], v[80:83], v[128:131], v[152:155]
	s_waitcnt lgkmcnt(3)
	s_barrier
	v_mfma_f32_16x16x32_bf16 v[196:199], v[132:135], v[204:207], v[196:199]
	v_mfma_f32_16x16x32_bf16 v[48:51], v[96:99], v[4:7], 0
	v_mfma_f32_16x16x32_bf16 v[88:91], v[24:27], v[100:103], v[88:91]
	v_mfma_f32_16x16x32_bf16 v[48:51], v[136:139], v[16:19], v[48:51]
	v_mfma_f32_16x16x32_bf16 v[88:91], v[140:143], v[184:187], v[88:91]
	s_nop 3
	v_add_f32_dpp v196, v152, v196 quad_perm:[0,1,2,3] row_mask:0xf bank_mask:0x3
	v_add_f32_dpp v197, v152, v197 row_ror:8 row_mask:0xf bank_mask:0x3
	v_mfma_f32_16x16x32_bf16 v[48:51], v[44:47], v[28:31], v[48:51]
	v_add_f32_dpp v198, v154, v198 quad_perm:[0,1,2,3] row_mask:0xf bank_mask:0x3
	v_add_f32_dpp v199, v154, v199 row_ror:8 row_mask:0xf bank_mask:0x3
	v_mfma_f32_16x16x32_bf16 v[88:91], v[108:111], v[192:195], v[88:91]
	ds_read_b128 v[80:83], v32 offset:3264
	v_add_f32_dpp v196, v153, v196 row_ror:8 row_mask:0xf bank_mask:0xc
	v_mfma_f32_16x16x32_bf16 v[48:51], v[124:127], v[52:55], v[48:51]
	ds_read_b128 v[112:115], v32 offset:3200
	v_add_f32_dpp v197, v153, v197 quad_perm:[0,1,2,3] row_mask:0xf bank_mask:0xc
	v_mfma_f32_16x16x32_bf16 v[88:91], v[8:11], v[212:215], v[88:91]
	ds_read_b128 v[164:167], v32 offset:3136
	v_add_f32_dpp v198, v155, v198 row_ror:8 row_mask:0xf bank_mask:0xc
	v_mfma_f32_16x16x32_bf16 v[48:51], v[188:191], v[56:59], v[48:51]
	ds_read_b128 v[204:207], v32 offset:3072
	v_add_f32_dpp v199, v155, v199 quad_perm:[0,1,2,3] row_mask:0xf bank_mask:0xc
	v_mfma_f32_16x16x32_bf16 v[88:91], v[116:119], v[92:95], v[88:91]
	ds_read_b64_tr_b16 v[92:93], v34 offset:10176
	v_sub_f32 v196, v196, v197
	v_mfma_f32_16x16x32_bf16 v[48:51], v[208:211], v[60:63], v[48:51]
	ds_read_b64_tr_b16 v[94:95], v34 offset:13912
	v_sub_f32 v198, v198, v199
	v_mfma_f32_16x16x32_bf16 v[88:91], v[160:163], v[76:79], v[88:91]
	ds_read_b128 v[152:155], v32 offset:1216
	v_fmac_f32 v197, v156, v196
	v_mfma_f32_16x16x32_bf16 v[48:51], v[84:87], v[64:67], v[48:51]
	ds_read_b128 v[84:87], v32 offset:3520
	v_fmac_f32 v199, v157, v198
	v_mfma_f32_16x16x32_bf16 v[88:91], v[168:171], v[20:23], v[88:91]
	ds_read_b64_tr_b16 v[208:209], v33 offset:11048
	v_cvt_pk_bf16_f32 v196, v197, v199
	v_mfma_f32_16x16x32_bf16 v[48:51], v[72:75], v[68:71], v[48:51]
	ds_write_addtid_b32 v196 offset:59392
	v_mfma_f32_16x16x32_bf16 v[88:91], v[176:179], v[120:123], v[88:91]
	ds_read_b64_tr_b16 v[210:211], v33 offset:7296
	v_mfma_f32_16x16x32_bf16 v[48:51], v[200:203], v[12:15], v[48:51]
	s_waitcnt lgkmcnt(1)
	s_barrier
	v_mfma_f32_16x16x32_bf16 v[88:91], v[224:227], v[172:175], v[88:91]
	v_mfma_f32_16x16x32_bf16 v[48:51], v[36:39], v[128:131], v[48:51]
	v_mfma_f32_16x16x32_bf16 v[88:91], v[132:135], v[236:239], v[88:91]
	v_mfma_f32_16x16x32_bf16 v[36:39], v[80:83], v[4:7], 0
	v_mfma_f32_16x16x32_bf16 v[180:183], v[24:27], v[144:147], v[180:183]
	v_mfma_f32_16x16x32_bf16 v[36:39], v[112:115], v[16:19], v[36:39]
	v_mfma_f32_16x16x32_bf16 v[180:183], v[140:143], v[40:43], v[180:183]
	ds_read_b128 v[72:75], v32 offset:3456
	s_nop 2
	v_add_f32_dpp v88, v48, v88 quad_perm:[0,1,2,3] row_mask:0xf bank_mask:0x3
	v_mfma_f32_16x16x32_bf16 v[36:39], v[164:167], v[28:31], v[36:39]
	ds_read_b128 v[196:199], v35 offset:6144
	v_add_f32_dpp v89, v48, v89 row_ror:8 row_mask:0xf bank_mask:0x3
	v_mfma_f32_16x16x32_bf16 v[180:183], v[108:111], v[100:103], v[180:183]
	ds_read_b64_tr_b16 v[200:201], v33 offset:10920
	v_add_f32_dpp v90, v50, v90 quad_perm:[0,1,2,3] row_mask:0xf bank_mask:0x3
	v_mfma_f32_16x16x32_bf16 v[36:39], v[204:207], v[52:55], v[36:39]
	ds_read_b64_tr_b16 v[202:203], v33 offset:7168
	v_add_f32_dpp v91, v50, v91 row_ror:8 row_mask:0xf bank_mask:0x3
	v_mfma_f32_16x16x32_bf16 v[180:183], v[8:11], v[184:187], v[180:183]
	ds_read_b128 v[184:187], v32 offset:3392
	v_add_f32_dpp v88, v49, v88 row_ror:8 row_mask:0xf bank_mask:0xc
	v_mfma_f32_16x16x32_bf16 v[36:39], v[96:99], v[56:59], v[36:39]
	ds_read_b128 v[212:215], v32 offset:3328
	v_add_f32_dpp v89, v49, v89 quad_perm:[0,1,2,3] row_mask:0xf bank_mask:0xc
	v_mfma_f32_16x16x32_bf16 v[180:183], v[116:119], v[192:195], v[180:183]
	ds_read_b64_tr_b16 v[192:193], v34 offset:10432
	v_add_f32_dpp v90, v51, v90 row_ror:8 row_mask:0xf bank_mask:0xc
	v_mfma_f32_16x16x32_bf16 v[36:39], v[136:139], v[60:63], v[36:39]
	ds_read_b64_tr_b16 v[194:195], v34 offset:14168
	v_add_f32_dpp v91, v51, v91 quad_perm:[0,1,2,3] row_mask:0xf bank_mask:0xc
	v_mfma_f32_16x16x32_bf16 v[180:183], v[160:163], v[92:95], v[180:183]
	ds_read_b128 v[48:51], v32 offset:1472
	v_sub_f32 v88, v88, v89
	v_mfma_f32_16x16x32_bf16 v[36:39], v[44:47], v[64:67], v[36:39]
	ds_read_b128 v[44:47], v32 offset:3776
	v_sub_f32 v90, v90, v91
	v_mfma_f32_16x16x32_bf16 v[180:183], v[168:171], v[76:79], v[180:183]
	ds_read_b128 v[136:139], v32 offset:3712
	v_fmac_f32 v89, v158, v88
	v_mfma_f32_16x16x32_bf16 v[36:39], v[124:127], v[68:71], v[36:39]
	ds_read_b128 v[124:127], v32 offset:3648
	v_fmac_f32 v91, v159, v90
	v_mfma_f32_16x16x32_bf16 v[180:183], v[176:179], v[20:23], v[180:183]
	ds_read_b128 v[216:219], v32 offset:3584
	v_cvt_pk_bf16_f32 v88, v89, v91
	v_mfma_f32_16x16x32_bf16 v[36:39], v[188:191], v[12:15], v[36:39]
	ds_write_addtid_b32 v88 offset:60416
	v_mfma_f32_16x16x32_bf16 v[180:183], v[224:227], v[120:123], v[180:183]
	v_mfma_f32_16x16x32_bf16 v[36:39], v[152:155], v[128:131], v[36:39]
	s_waitcnt lgkmcnt(0)
	s_barrier
	v_mfma_f32_16x16x32_bf16 v[180:183], v[132:135], v[172:175], v[180:183]
	v_mfma_f32_16x16x32_bf16 v[88:91], v[84:87], v[4:7], 0
	ds_read_b64_tr_b16 v[152:153], v33 offset:11304
	v_mfma_f32_16x16x32_bf16 v[240:243], v[24:27], v[208:211], v[240:243]
	ds_read_b64_tr_b16 v[154:155], v33 offset:7552
	v_mfma_f32_16x16x32_bf16 v[88:91], v[72:75], v[16:19], v[88:91]
	ds_read_b64_tr_b16 v[156:157], v33 offset:11176
	v_mfma_f32_16x16x32_bf16 v[240:243], v[140:143], v[200:203], v[240:243]
	ds_read_b64_tr_b16 v[158:159], v33 offset:7424
	v_add_f32_dpp v180, v36, v180 quad_perm:[0,1,2,3] row_mask:0xf bank_mask:0x3
	v_mfma_f32_16x16x32_bf16 v[88:91], v[184:187], v[28:31], v[88:91]
	ds_read_b64_tr_b16 v[172:173], v34 offset:10688
	v_add_f32_dpp v181, v36, v181 row_ror:8 row_mask:0xf bank_mask:0x3
	v_mfma_f32_16x16x32_bf16 v[240:243], v[108:111], v[144:147], v[240:243]
	ds_read_b64_tr_b16 v[174:175], v34 offset:14424
	v_add_f32_dpp v182, v38, v182 quad_perm:[0,1,2,3] row_mask:0xf bank_mask:0x3
	v_mfma_f32_16x16x32_bf16 v[88:91], v[212:215], v[52:55], v[88:91]
	ds_read_b128 v[188:191], v32 offset:1728
	v_add_f32_dpp v183, v38, v183 row_ror:8 row_mask:0xf bank_mask:0x3
	v_mfma_f32_16x16x32_bf16 v[240:243], v[8:11], v[40:43], v[240:243]
	v_add_f32_dpp v180, v37, v180 row_ror:8 row_mask:0xf bank_mask:0xc
	v_add_f32_dpp v181, v37, v181 quad_perm:[0,1,2,3] row_mask:0xf bank_mask:0xc
	v_mfma_f32_16x16x32_bf16 v[88:91], v[80:83], v[56:59], v[88:91]
	ds_read_b128 v[40:43], v32 offset:4032
	v_add_f32_dpp v182, v39, v182 row_ror:8 row_mask:0xf bank_mask:0xc
	v_mfma_f32_16x16x32_bf16 v[240:243], v[116:119], v[100:103], v[240:243]
	ds_read_b64_tr_b16 v[100:101], v33 offset:11560
	v_add_f32_dpp v183, v39, v183 quad_perm:[0,1,2,3] row_mask:0xf bank_mask:0xc
	v_mfma_f32_16x16x32_bf16 v[88:91], v[112:115], v[60:63], v[88:91]
	ds_read_b64_tr_b16 v[102:103], v33 offset:7808
	v_sub_f32 v180, v180, v181
	v_mfma_f32_16x16x32_bf16 v[240:243], v[160:163], v[192:195], v[240:243]
	ds_read_b128 v[36:39], v32 offset:3968
	v_sub_f32 v182, v182, v183
	v_mfma_f32_16x16x32_bf16 v[88:91], v[164:167], v[64:67], v[88:91]
	ds_read_b128 v[112:115], v35 offset:7168
	v_fmac_f32 v181, v196, v180
	v_mfma_f32_16x16x32_bf16 v[240:243], v[168:171], v[92:95], v[240:243]
	ds_read_b64_tr_b16 v[164:165], v33 offset:11432
	v_fmac_f32 v183, v197, v182
	v_mfma_f32_16x16x32_bf16 v[88:91], v[204:207], v[68:71], v[88:91]
	ds_read_b64_tr_b16 v[166:167], v33 offset:7680
	v_cvt_pk_bf16_f32 v180, v181, v183
	v_mfma_f32_16x16x32_bf16 v[240:243], v[176:179], v[76:79], v[240:243]
	ds_write_addtid_b32 v180 offset:61440
	v_mfma_f32_16x16x32_bf16 v[88:91], v[96:99], v[12:15], v[88:91]
	ds_read_b128 v[96:99], v32 offset:3904
	v_mfma_f32_16x16x32_bf16 v[240:243], v[224:227], v[20:23], v[240:243]
	s_waitcnt lgkmcnt(1)
	s_barrier
	v_mfma_f32_16x16x32_bf16 v[88:91], v[48:51], v[128:131], v[88:91]
	v_mfma_f32_16x16x32_bf16 v[240:243], v[132:135], v[120:123], v[240:243]
	ds_read_b128 v[48:51], v32 offset:3840
	v_mfma_f32_16x16x32_bf16 v[120:123], v[44:47], v[4:7], 0
	ds_read_b64_tr_b16 v[180:181], v34 offset:10944
	v_mfma_f32_16x16x32_bf16 v[104:107], v[24:27], v[152:155], v[104:107]
	ds_read_b64_tr_b16 v[182:183], v34 offset:14680
	v_mfma_f32_16x16x32_bf16 v[120:123], v[136:139], v[16:19], v[120:123]
	v_mfma_f32_16x16x32_bf16 v[104:107], v[140:143], v[156:159], v[104:107]
	s_nop 0
	v_add_f32_dpp v240, v88, v240 quad_perm:[0,1,2,3] row_mask:0xf bank_mask:0x3
	v_add_f32_dpp v241, v88, v241 row_ror:8 row_mask:0xf bank_mask:0x3
	v_mfma_f32_16x16x32_bf16 v[120:123], v[124:127], v[28:31], v[120:123]
	v_add_f32_dpp v242, v90, v242 quad_perm:[0,1,2,3] row_mask:0xf bank_mask:0x3
	v_add_f32_dpp v243, v90, v243 row_ror:8 row_mask:0xf bank_mask:0x3
	v_mfma_f32_16x16x32_bf16 v[104:107], v[108:111], v[208:211], v[104:107]
	v_add_f32_dpp v240, v89, v240 row_ror:8 row_mask:0xf bank_mask:0xc
	v_add_f32_dpp v241, v89, v241 quad_perm:[0,1,2,3] row_mask:0xf bank_mask:0xc
	v_mfma_f32_16x16x32_bf16 v[120:123], v[216:219], v[52:55], v[120:123]
	ds_read_b128 v[204:207], v32 offset:1984
	v_add_f32_dpp v242, v91, v242 row_ror:8 row_mask:0xf bank_mask:0xc
	v_mfma_f32_16x16x32_bf16 v[104:107], v[8:11], v[200:203], v[104:107]
	v_add_f32_dpp v243, v91, v243 quad_perm:[0,1,2,3] row_mask:0xf bank_mask:0xc
	v_sub_f32 v240, v240, v241
	v_mfma_f32_16x16x32_bf16 v[120:123], v[84:87], v[56:59], v[120:123]
	v_sub_f32 v242, v242, v243
	v_fmac_f32 v241, v198, v240
	v_mfma_f32_16x16x32_bf16 v[104:107], v[116:119], v[144:147], v[104:107]
	v_fmac_f32 v243, v199, v242
	v_cvt_pk_bf16_f32 v240, v241, v243
	v_mfma_f32_16x16x32_bf16 v[120:123], v[72:75], v[60:63], v[120:123]
	ds_write_addtid_b32 v240 offset:62464
	v_mfma_f32_16x16x32_bf16 v[104:107], v[160:163], v[172:175], v[104:107]
	v_mfma_f32_16x16x32_bf16 v[120:123], v[184:187], v[64:67], v[120:123]
	s_waitcnt lgkmcnt(0)
	s_barrier
	v_mfma_f32_16x16x32_bf16 v[104:107], v[168:171], v[192:195], v[104:107]
	v_mfma_f32_16x16x32_bf16 v[120:123], v[212:215], v[68:71], v[120:123]
	v_mfma_f32_16x16x32_bf16 v[104:107], v[176:179], v[92:95], v[104:107]
	v_mfma_f32_16x16x32_bf16 v[120:123], v[80:83], v[12:15], v[120:123]
	v_mfma_f32_16x16x32_bf16 v[104:107], v[224:227], v[76:79], v[104:107]
	v_mfma_f32_16x16x32_bf16 v[120:123], v[188:191], v[128:131], v[120:123]
	v_mfma_f32_16x16x32_bf16 v[104:107], v[132:135], v[20:23], v[104:107]
	v_mfma_f32_16x16x32_bf16 v[20:23], v[40:43], v[4:7], 0
	v_mfma_f32_16x16x32_bf16 v[148:151], v[24:27], v[100:103], v[148:151]
	v_mfma_f32_16x16x32_bf16 v[20:23], v[36:39], v[16:19], v[20:23]
	v_mfma_f32_16x16x32_bf16 v[148:151], v[140:143], v[164:167], v[148:151]
	s_nop 3
	v_add_f32_dpp v104, v120, v104 quad_perm:[0,1,2,3] row_mask:0xf bank_mask:0x3
	v_add_f32_dpp v105, v120, v105 row_ror:8 row_mask:0xf bank_mask:0x3
	v_add_f32_dpp v106, v122, v106 quad_perm:[0,1,2,3] row_mask:0xf bank_mask:0x3
	v_mfma_f32_16x16x32_bf16 v[20:23], v[96:99], v[28:31], v[20:23]
	v_add_f32_dpp v107, v122, v107 row_ror:8 row_mask:0xf bank_mask:0x3
	v_add_f32_dpp v104, v121, v104 row_ror:8 row_mask:0xf bank_mask:0xc
	v_mfma_f32_16x16x32_bf16 v[148:151], v[108:111], v[152:155], v[148:151]
	v_add_f32_dpp v105, v121, v105 quad_perm:[0,1,2,3] row_mask:0xf bank_mask:0xc
	v_add_f32_dpp v106, v123, v106 row_ror:8 row_mask:0xf bank_mask:0xc
	v_mfma_f32_16x16x32_bf16 v[20:23], v[48:51], v[52:55], v[20:23]
	v_add_f32_dpp v107, v123, v107 quad_perm:[0,1,2,3] row_mask:0xf bank_mask:0xc
	v_sub_f32 v104, v104, v105
	v_mfma_f32_16x16x32_bf16 v[148:151], v[8:11], v[156:159], v[148:151]
	v_sub_f32 v106, v106, v107
	v_fmac_f32 v105, v112, v104
	v_mfma_f32_16x16x32_bf16 v[20:23], v[44:47], v[56:59], v[20:23]
	v_fmac_f32 v107, v113, v106
	v_cvt_pk_bf16_f32 v104, v105, v107
	v_mfma_f32_16x16x32_bf16 v[148:151], v[116:119], v[208:211], v[148:151]
	ds_write_addtid_b32 v104 offset:63488
	v_mfma_f32_16x16x32_bf16 v[20:23], v[136:139], v[60:63], v[20:23]
	v_mfma_f32_16x16x32_bf16 v[148:151], v[160:163], v[180:183], v[148:151]
	s_waitcnt lgkmcnt(0)
	s_barrier
	v_mfma_f32_16x16x32_bf16 v[20:23], v[124:127], v[64:67], v[20:23]
	v_mfma_f32_16x16x32_bf16 v[148:151], v[168:171], v[172:175], v[148:151]
	v_mfma_f32_16x16x32_bf16 v[20:23], v[216:219], v[68:71], v[20:23]
	v_mfma_f32_16x16x32_bf16 v[148:151], v[176:179], v[192:195], v[148:151]
	v_mfma_f32_16x16x32_bf16 v[20:23], v[84:87], v[12:15], v[20:23]
	v_mfma_f32_16x16x32_bf16 v[148:151], v[224:227], v[92:95], v[148:151]
	v_mfma_f32_16x16x32_bf16 v[20:23], v[204:207], v[128:131], v[20:23]
	v_mfma_f32_16x16x32_bf16 v[148:151], v[132:135], v[76:79], v[148:151]
	s_nop 7
	v_add_f32_dpp v148, v20, v148 quad_perm:[0,1,2,3] row_mask:0xf bank_mask:0x3
	v_add_f32_dpp v149, v20, v149 row_ror:8 row_mask:0xf bank_mask:0x3
	v_add_f32_dpp v150, v22, v150 quad_perm:[0,1,2,3] row_mask:0xf bank_mask:0x3
	v_add_f32_dpp v151, v22, v151 row_ror:8 row_mask:0xf bank_mask:0x3
	v_add_f32_dpp v148, v21, v148 row_ror:8 row_mask:0xf bank_mask:0xc
	v_add_f32_dpp v149, v21, v149 quad_perm:[0,1,2,3] row_mask:0xf bank_mask:0xc
	v_add_f32_dpp v150, v23, v150 row_ror:8 row_mask:0xf bank_mask:0xc
	v_add_f32_dpp v151, v23, v151 quad_perm:[0,1,2,3] row_mask:0xf bank_mask:0xc
	v_sub_f32 v148, v148, v149
	v_sub_f32 v150, v150, v151
	v_fmac_f32 v149, v114, v148
	v_fmac_f32 v151, v115, v150
	v_cvt_pk_bf16_f32 v148, v149, v151
	ds_write_addtid_b32 v148 offset:64512
	s_waitcnt lgkmcnt(0)
	s_barrier
	s_endpgm
.Lk_partner:
	s_setprio 2
	s_lshr_b32 s42, s36, 3
	s_lshl_b32 s42, s42, 7
	s_and_b32 s43, s36, 7
	s_lshl3_add_u32 s42, s43, s42
	s_add_i32 s42, s42, 32768
	v_mov_b32 v60, s42
	s_mov_b32 s54, 0
	global_load_dwordx2 v[26:27], v60, s[28:29] sc1
	s_add_u32 s44, s56, 0x2000
	s_addc_u32 s45, s57, 0
	global_load_dwordx4 v[216:219], v29, s[56:57] offset:0 sc1
	global_load_dwordx4 v[220:223], v29, s[56:57] offset:16 sc1
	global_load_dwordx4 v[224:227], v29, s[44:45] offset:0 sc1
	global_load_dwordx4 v[228:231], v29, s[44:45] offset:16 sc1
	ds_read_b128 v[80:83], v30 offset:0
	ds_read_b128 v[84:87], v30 offset:32
	ds_read_b128 v[88:91], v30 offset:64
	ds_read_b128 v[92:95], v30 offset:96
	ds_read_b128 v[96:99], v30 offset:128
	ds_read_b128 v[100:103], v30 offset:160
	ds_read_b128 v[104:107], v30 offset:192
	ds_read_b128 v[108:111], v30 offset:224
	s_waitcnt vmcnt(11)
	v_readfirstlane_b32 s38, v52
	v_readfirstlane_b32 s39, v53
	s_add_u32 s38, s38, 0x7f4a7c15
	s_addc_u32 s39, s39, 0x9e3779b9
	s_add_i32 s42, s6, 0
	s_and_b32 s42, s42, 3
	s_lshr_b32 s43, s42, 1
	s_mul_i32 s43, s43, 4352
	s_and_b32 s42, s42, 1
	s_lshl_b32 s42, s42, 9
	s_add_i32 s42, s42, s43
	v_add_u32 v56, s42, v28
	s_add_i32 s42, s6, 1
	s_and_b32 s42, s42, 3
	s_lshr_b32 s43, s42, 1
	s_mul_i32 s43, s43, 4352
	s_and_b32 s42, s42, 1
	s_lshl_b32 s42, s42, 9
	s_add_i32 s42, s42, s43
	v_add_u32 v57, s42, v28
	s_add_i32 s42, s6, 2
	s_and_b32 s42, s42, 3
	s_lshr_b32 s43, s42, 1
	s_mul_i32 s43, s43, 4352
	s_and_b32 s42, s42, 1
	s_lshl_b32 s42, s42, 9
	s_add_i32 s42, s42, s43
	v_add_u32 v58, s42, v28
	s_add_i32 s42, s6, 3
	s_and_b32 s42, s42, 3
	s_lshr_b32 s43, s42, 1
	s_mul_i32 s43, s43, 4352
	s_and_b32 s42, s42, 1
	s_lshl_b32 s42, s42, 9
	s_add_i32 s42, s42, s43
	v_add_u32 v59, s42, v28
	ds_write_b64 v56, v[2:3] offset:64
	ds_write_b64 v57, v[4:5] offset:64
	s_mov_b64 exec, 0xff
	ds_write_b64 v28, v[6:7] offset:0
	s_mov_b64 exec, -1
	ds_write_b64 v58, v[8:9] offset:64
	ds_write_b64 v59, v[10:11] offset:64
	s_mov_b64 exec, 0xff
	ds_write_b64 v28, v[12:13] offset:4352
	s_mov_b64 exec, -1
	ds_read_b64_tr_b16 v[166:167], v54 offset:64
	ds_read_b64_tr_b16 v[164:165], v54 offset:32
	ds_read_b64_tr_b16 v[168:169], v54 offset:96
	ds_read_b64_tr_b16 v[162:163], v54 offset:0
	ds_read_b64_tr_b16 v[170:171], v54 offset:48
	ds_read_b64_tr_b16 v[172:173], v54 offset:80
	s_waitcnt lgkmcnt(5)
	v_lshlrev_b32 v152, 16, v166
	v_mfma_f32_32x32x16_f16 v[64:79], v[80:83], v[112:115], v[64:79]
	s_waitcnt lgkmcnt(4)
	v_and_b32 v150, s60, v165
	v_and_b32 v153, s60, v166
	v_lshlrev_b32 v149, 16, v165
	v_add_f32 v153, v153, v152
	v_add_f32 v149, v150, v149
	v_lshlrev_b32 v154, 16, v167
	v_mfma_f32_32x32x16_f16 v[64:79], v[84:87], v[116:119], v[64:79]
	v_and_b32 v148, s60, v164
	v_add_f32 v154, v154, v153
	v_add_f32 v148, v149, v148
	v_and_b32 v155, s60, v167
	v_lshlrev_b32 v147, 16, v164
	v_add_f32 v155, v155, v154
	v_mfma_f32_32x32x16_f16 v[64:79], v[88:91], v[120:123], v[64:79]
	v_add_f32 v147, v148, v147
	s_waitcnt lgkmcnt(3)
	v_lshlrev_b32 v156, 16, v168
	s_waitcnt lgkmcnt(2)
	v_and_b32 v146, s60, v163
	v_add_f32 v156, v156, v155
	v_add_f32 v146, v147, v146
	v_and_b32 v157, s60, v168
	v_mfma_f32_32x32x16_f16 v[64:79], v[92:95], v[124:127], v[64:79]
	v_lshlrev_b32 v145, 16, v163
	v_add_f32 v157, v157, v156
	v_add_f32 v145, v146, v145
	v_lshlrev_b32 v158, 16, v169
	v_and_b32 v144, s60, v162
	v_add_f32 v158, v158, v157
	v_mfma_f32_32x32x16_f16 v[64:79], v[96:99], v[128:131], v[64:79]
	v_add_f32 v144, v145, v144
	v_sub_f32 v190, v146, v149
	v_sub_f32 v191, v147, v150
	v_add_f32 v193, v152, v149
	v_add_f32 v194, v153, v150
	v_sub_f32 v196, v155, v152
	v_mfma_f32_32x32x16_f16 v[64:79], v[100:103], v[132:135], v[64:79]
	v_sub_f32 v197, v156, v153
	v_cvt_pk_bf16_f32 v178, v190, v191
	v_cvt_pk_bf16_f32 v179, v148, v193
	v_cvt_pk_bf16_f32 v180, v194, v154
	v_cvt_pk_bf16_f32 v181, v196, v197
	v_sub_f32 v190, v145, v150
	v_mfma_f32_32x32x16_f16 v[64:79], v[104:107], v[136:139], v[64:79]
	v_add_f32 v192, v152, v147
	v_add_f32 v193, v153, v148
	v_add_f32 v194, v154, v149
	v_add_f32 v195, v155, v150
	v_sub_f32 v197, v157, v152
	v_cvt_pk_bf16_f32 v182, v190, v146
	v_mfma_f32_32x32x16_f16 v[64:79], v[108:111], v[140:143], v[64:79]
	v_cvt_pk_bf16_f32 v183, v192, v193
	v_cvt_pk_bf16_f32 v184, v194, v195
	v_cvt_pk_bf16_f32 v185, v156, v197
	v_add_f32 v191, v152, v145
	v_add_f32 v192, v153, v146
	v_add_f32 v193, v154, v147
	v_add_f32 v194, v155, v148
	v_add_f32 v195, v156, v149
	v_add_f32 v196, v157, v150
	v_cvt_pk_bf16_f32 v186, v144, v191
	v_cvt_pk_bf16_f32 v187, v192, v193
	v_cvt_pk_bf16_f32 v188, v194, v195
	v_cvt_pk_bf16_f32 v189, v196, v158
	ds_read_b64_tr_b16 v[84:85], v54 offset:4416
	ds_read_b64_tr_b16 v[82:83], v54 offset:4384
	ds_read_b64_tr_b16 v[86:87], v54 offset:4448
	ds_read_b64_tr_b16 v[80:81], v54 offset:4352
	ds_read_b64_tr_b16 v[88:89], v54 offset:4400
	ds_read_b64_tr_b16 v[90:91], v54 offset:4432
	s_mul_i32 s42, s36, 3072
	s_add_i32 s42, s42, 65536
	v_lshl_add_u32 v61, v1, 4, s42
	s_waitcnt vmcnt(4)
.Lk_fchk:
	v_cmp_ne_u64 vcc, s[38:39], v[26:27]
	s_cbranch_vccnz .Lk_fslow
.Lk_fok:
	global_load_dwordx4 v[112:115], v61, s[28:29] offset:0 sc1
	global_load_dwordx4 v[116:119], v61, s[28:29] offset:1024 sc1
	global_load_dwordx4 v[120:123], v61, s[28:29] offset:2048 sc1
	ds_write_b64 v56, v[14:15] offset:8768
	ds_write_b64 v57, v[16:17] offset:8768
	s_mov_b64 exec, 0xff
	ds_write_b64 v28, v[18:19] offset:8704
	s_mov_b64 exec, -1
	ds_write_b64 v58, v[20:21] offset:8768
	ds_write_b64 v59, v[22:23] offset:8768
	s_mov_b64 exec, 0xff
	ds_write_b64 v28, v[24:25] offset:13056
	s_mov_b64 exec, -1
	s_waitcnt lgkmcnt(11)
	v_lshlrev_b32 v152, 16, v84
	s_waitcnt lgkmcnt(10)
	v_and_b32 v150, s60, v83
	v_and_b32 v153, s60, v84
	v_lshlrev_b32 v149, 16, v83
	v_add_f32 v153, v153, v152
	v_add_f32 v149, v150, v149
	v_lshlrev_b32 v154, 16, v85
	v_and_b32 v148, s60, v82
	v_add_f32 v154, v154, v153
	v_add_f32 v148, v149, v148
	v_and_b32 v155, s60, v85
	v_lshlrev_b32 v147, 16, v82
	v_add_f32 v155, v155, v154
	v_add_f32 v147, v148, v147
	s_waitcnt lgkmcnt(9)
	v_lshlrev_b32 v156, 16, v86
	s_waitcnt lgkmcnt(8)
	v_and_b32 v146, s60, v81
	v_add_f32 v156, v156, v155
	v_add_f32 v146, v147, v146
	v_and_b32 v157, s60, v86
	v_lshlrev_b32 v145, 16, v81
	v_add_f32 v157, v157, v156
	v_add_f32 v145, v146, v145
	v_lshlrev_b32 v158, 16, v87
	v_and_b32 v144, s60, v80
	v_add_f32 v158, v158, v157
	v_add_f32 v144, v145, v144
	v_sub_f32 v190, v146, v149
	v_sub_f32 v191, v147, v150
	v_add_f32 v193, v152, v149
	v_add_f32 v194, v153, v150
	v_sub_f32 v196, v155, v152
	v_sub_f32 v197, v156, v153
	v_cvt_pk_bf16_f32 v92, v190, v191
	v_cvt_pk_bf16_f32 v93, v148, v193
	v_cvt_pk_bf16_f32 v94, v194, v154
	v_cvt_pk_bf16_f32 v95, v196, v197
	v_sub_f32 v190, v145, v150
	v_add_f32 v192, v152, v147
	v_add_f32 v193, v153, v148
	v_add_f32 v194, v154, v149
	v_add_f32 v195, v155, v150
	v_sub_f32 v197, v157, v152
	v_cvt_pk_bf16_f32 v96, v190, v146
	v_cvt_pk_bf16_f32 v97, v192, v193
	v_cvt_pk_bf16_f32 v98, v194, v195
	v_cvt_pk_bf16_f32 v99, v156, v197
	v_add_f32 v191, v152, v145
	v_add_f32 v192, v153, v146
	v_add_f32 v193, v154, v147
	v_add_f32 v194, v155, v148
	v_add_f32 v195, v156, v149
	v_add_f32 v196, v157, v150
	v_cvt_pk_bf16_f32 v100, v144, v191
	v_cvt_pk_bf16_f32 v101, v192, v193
	v_cvt_pk_bf16_f32 v102, v194, v195
	v_cvt_pk_bf16_f32 v103, v196, v158
	s_setprio 2
	s_waitcnt vmcnt(3)
.Lk_gchk:
	v_xor_b32 v215, s38, v216
	v_bitop3_b32 v215, v218, s38, v215 bitop3:0xbe
	v_bitop3_b32 v215, v220, s38, v215 bitop3:0xbe
	v_bitop3_b32 v215, v222, s38, v215 bitop3:0xbe
	v_bitop3_b32 v215, v224, s38, v215 bitop3:0xbe
	v_bitop3_b32 v215, v226, s38, v215 bitop3:0xbe
	v_bitop3_b32 v215, v228, s38, v215 bitop3:0xbe
	v_bitop3_b32 v215, v230, s38, v215 bitop3:0xbe
	v_and_b32 v215, 0xffff, v215
	v_bitop3_b32 v215, v217, s39, v215 bitop3:0xbe
	v_bitop3_b32 v215, v219, s39, v215 bitop3:0xbe
	v_bitop3_b32 v215, v221, s39, v215 bitop3:0xbe
	v_bitop3_b32 v215, v223, s39, v215 bitop3:0xbe
	v_bitop3_b32 v215, v225, s39, v215 bitop3:0xbe
	v_bitop3_b32 v215, v227, s39, v215 bitop3:0xbe
	v_bitop3_b32 v215, v229, s39, v215 bitop3:0xbe
	v_bitop3_b32 v215, v231, s39, v215 bitop3:0xbe
	v_cmp_ne_u32 vcc, 0, v215
	s_cbranch_vccnz .Lk_gslow
.Lk_gok:
	v_and_b32 v232, s60, v216
	v_and_b32 v233, s60, v218
	v_and_b32 v234, s60, v220
	v_and_b32 v235, s60, v222
	v_and_b32 v236, s60, v224
	v_and_b32 v237, s60, v226
	v_and_b32 v238, s60, v228
	v_and_b32 v239, s60, v230
	ds_write_b128 v252, v[232:235]
	ds_write_b128 v252, v[236:239] offset:4096
	s_barrier
	s_mov_b32 m0, s53
	s_nop 0
	ds_write_addtid_b32 v240
	s_mov_b32 exec_hi, 0xff
	ds_write_addtid_b32 v240 offset:4352
	ds_write_addtid_b32 v240 offset:8096
	ds_write_addtid_b32 v240 offset:11840
	ds_write_addtid_b32 v240 offset:15072
	s_mov_b64 exec, -1
	ds_write_b128 v55, v[166:169] offset:96
	s_waitcnt vmcnt(2)
	v_add_f32 v112, v112, v251
	s_waitcnt vmcnt(1)
	v_fma_f32 v68, v68, s48, v116
	v_fma_f32 v69, v69, s48, v117
	ds_write_b128 v55, v[170:173] offset:4352
	v_fma_f32 v70, v70, s48, v118
	v_fma_f32 v71, v71, s48, v119
	s_waitcnt vmcnt(0)
	v_fma_f32 v72, v72, s48, v120
	v_fma_f32 v73, v73, s48, v121
	ds_write_b128 v55, v[178:181] offset:8096
	v_fma_f32 v74, v74, s48, v122
	v_fma_f32 v75, v75, s48, v123
	v_fma_f32 v65, v65, s48, v113
	v_fma_f32 v66, v66, s48, v114
	ds_write_b128 v55, v[182:185] offset:11840
	v_fma_f32 v67, v67, s48, v115
	v_fma_f32 v64, v64, s48, v112
	v_cvt_pk_bf16_f32 v202, v68, v69
	v_cvt_pk_bf16_f32 v203, v70, v71
	ds_write_b128 v55, v[186:189] offset:15072
	v_cvt_pk_bf16_f32 v204, v72, v73
	v_cvt_pk_bf16_f32 v205, v74, v75
	v_cvt_pk_bf16_f32 v200, v67, v66
	v_cvt_pk_bf16_f32 v201, v65, v64
	ds_write_b128 v55, v[84:87] offset:1120
	v_readlane_b32 s42, v202, 32
	v_readlane_b32 s43, v204, 0
	v_mov_b32_dpp v208, v202 quad_perm:[0,0,0,0] row_mask:0xf bank_mask:0xf
	v_mov_b32_dpp v209, v203 quad_perm:[0,0,0,0] row_mask:0xf bank_mask:0xf
	s_waitcnt lgkmcnt(14)
	ds_write_b128 v55, v[88:91] offset:5376
	v_mov_b32_dpp v210, v204 quad_perm:[1,0,3,2] row_mask:0xf bank_mask:0xf
	v_mov_b32_dpp v211, v205 quad_perm:[1,0,3,2] row_mask:0xf bank_mask:0xf
	s_and_b32 s42, s42, 0xffff
	v_and_b32 v206, v250, v202
	ds_write_b128 v55, v[92:95] offset:9120
	v_alignbit_b32 v207, v201, v200, 16
	v_perm_b32 v212, v209, v208, v253
	v_perm_b32 v213, v209, v208, v249
	v_writelane_b32 v204, s43, 33
	ds_write_b128 v55, v[96:99] offset:12864
	v_writelane_b32 v206, s42, 0
	v_writelane_b32 v203, s42, 0
	v_perm_b32 v211, v211, v205, v254
	ds_write_b128 v55, v[100:103] offset:16096
	v_perm_b32 v210, v210, v204, v254
	s_add_i32 m0, s51, -64800
	s_mov_b64 exec, 15
	ds_write_addtid_b32 v212 offset:65456
	ds_write_addtid_b32 v213 offset:64848
	s_mov_b64 exec, -1
	ds_write_b64 v244, v[200:201]
	ds_write_b16 v244, v200 offset:606
	ds_write_b32 v244, v207 offset:608
	ds_write_b16_d16_hi v244, v201 offset:612
	ds_write_b32 v245, v206
	ds_write_b32 v246, v203
	s_mov_b32 exec_hi, 3
	ds_write_b32 v247, v210 offset:752
	s_mov_b32 exec_hi, 0
	ds_write_b32 v247, v211 offset:880
	s_mov_b64 exec, -1
	s_waitcnt lgkmcnt(0)
	s_barrier
	v_and_b32 v224, 3, v1
	v_bfe_u32 v225, v1, 2, 2
	v_lshrrev_b32 v226, 4, v1
	v_bfe_u32 v227, v1, 3, 1
	v_and_b32 v228, 7, v1
	s_lshl_b32 s42, s33, 10
	s_add_i32 s42, s42, 69248
	v_lshl_add_u32 v32, v1, 2, s42
	v_lshlrev_b32 v62, 6, v227
	v_lshl_add_u32 v62, v226, 4, v62
	v_add_u32 v62, v62, v228
	v_mul_u32_u24 v62, 0x500, v62
	s_mul_i32 s42, s33, 163840
	s_lshl1_add_u32 s42, s35, s42
	v_add_u32 v33, s42, v62
	v_add_u32 v34, 0x2800, v33
	s_setprio 0
	s_cmp_eq_u32 s33, 3
	s_cbranch_scc0 .Lk_nack
	s_lshl_b32 s42, s37, 3
	s_add_i32 s42, s42, 4096
	v_mov_b32 v136, s42
	v_mov_b32 v138, s38
	v_mov_b32 v139, s39
	s_mov_b64 exec, 1
	global_store_dwordx2 v136, v[138:139], s[28:29] sc1
	s_mov_b64 exec, -1
.Lk_nack:
	s_waitcnt lgkmcnt(0)
	s_barrier
	s_cmp_le_u32 s33, 1
	s_cbranch_scc0 .Lk_o0
	ds_read2st64_b32 v[116:117], v32 offset0:0 offset1:1
	ds_read2st64_b32 v[118:119], v32 offset0:2 offset1:3
	s_waitcnt lgkmcnt(1)
	v_perm_b32 v132, v117, v116, s58
	s_waitcnt lgkmcnt(0)
	v_perm_b32 v133, v119, v118, s58
	global_store_dwordx2 v33, v[132:133], s[26:27]
	v_perm_b32 v134, v117, v116, s59
	v_perm_b32 v135, v119, v118, s59
	global_store_dwordx2 v34, v[134:135], s[26:27]
	s_add_u32 s26, s26, 0xa0000
	s_addc_u32 s27, s27, 0
.Lk_o0:
	s_waitcnt lgkmcnt(0)
	s_barrier
	s_cmp_ge_u32 s33, 2
	s_cbranch_scc0 .Lk_o2
	ds_read2st64_b32 v[116:117], v32 offset0:0 offset1:1
	ds_read2st64_b32 v[118:119], v32 offset0:2 offset1:3
	s_waitcnt lgkmcnt(1)
	v_perm_b32 v132, v117, v116, s58
	s_waitcnt lgkmcnt(0)
	v_perm_b32 v133, v119, v118, s58
	global_store_dwordx2 v33, v[132:133], s[26:27]
	v_perm_b32 v134, v117, v116, s59
	v_perm_b32 v135, v119, v118, s59
	global_store_dwordx2 v34, v[134:135], s[26:27]
	s_add_u32 s26, s26, 0xa0000
	s_addc_u32 s27, s27, 0
.Lk_o2:
	ds_read_b64_tr_b16 v[166:167], v54 offset:8768
	ds_read_b64_tr_b16 v[164:165], v54 offset:8736
	ds_read_b64_tr_b16 v[168:169], v54 offset:8800
	ds_read_b64_tr_b16 v[162:163], v54 offset:8704
	ds_read_b64_tr_b16 v[170:171], v54 offset:8752
	ds_read_b64_tr_b16 v[172:173], v54 offset:8784
	s_waitcnt lgkmcnt(0)
	s_barrier
	s_cmp_le_u32 s33, 1
	s_cbranch_scc0 .Lk_o4
	ds_read2st64_b32 v[116:117], v32 offset0:16 offset1:17
	ds_read2st64_b32 v[118:119], v32 offset0:18 offset1:19
	s_waitcnt lgkmcnt(1)
	v_perm_b32 v132, v117, v116, s58
	s_waitcnt lgkmcnt(0)
	v_perm_b32 v133, v119, v118, s58
	global_store_dwordx2 v33, v[132:133], s[26:27]
	v_perm_b32 v134, v117, v116, s59
	v_perm_b32 v135, v119, v118, s59
	global_store_dwordx2 v34, v[134:135], s[26:27]
	s_add_u32 s26, s26, 0xa0000
	s_addc_u32 s27, s27, 0
.Lk_o4:
	ds_write_b128 v55, v[166:169] offset:2144
	v_lshlrev_b32 v152, 16, v166
	v_and_b32 v150, s60, v165
	v_and_b32 v153, s60, v166
	v_lshlrev_b32 v149, 16, v165
	v_add_f32 v153, v153, v152
	v_add_f32 v149, v150, v149
	v_lshlrev_b32 v154, 16, v167
	v_and_b32 v148, s60, v164
	v_add_f32 v154, v154, v153
	v_add_f32 v148, v149, v148
	v_and_b32 v155, s60, v167
	v_lshlrev_b32 v147, 16, v164
	v_add_f32 v155, v155, v154
	v_add_f32 v147, v148, v147
	v_lshlrev_b32 v156, 16, v168
	v_and_b32 v146, s60, v163
	v_add_f32 v156, v156, v155
	v_add_f32 v146, v147, v146
	v_and_b32 v157, s60, v168
	v_lshlrev_b32 v145, 16, v163
	v_add_f32 v157, v157, v156
	v_add_f32 v145, v146, v145
	v_sub_f32 v190, v146, v149
	v_sub_f32 v191, v147, v150
	v_add_f32 v193, v152, v149
	v_add_f32 v194, v153, v150
	v_sub_f32 v196, v155, v152
	v_sub_f32 v197, v156, v153
	v_cvt_pk_bf16_f32 v178, v190, v191
	v_cvt_pk_bf16_f32 v179, v148, v193
	v_cvt_pk_bf16_f32 v180, v194, v154
	v_cvt_pk_bf16_f32 v181, v196, v197
	s_waitcnt lgkmcnt(0)
	s_barrier
	ds_write_b128 v55, v[170:173] offset:6400
	ds_write_b128 v55, v[178:181] offset:10144
	v_sub_f32 v190, v145, v150
	v_add_f32 v192, v152, v147
	v_add_f32 v193, v153, v148
	v_add_f32 v194, v154, v149
	v_add_f32 v195, v155, v150
	v_sub_f32 v197, v157, v152
	v_cvt_pk_bf16_f32 v182, v190, v146
	v_cvt_pk_bf16_f32 v183, v192, v193
	v_cvt_pk_bf16_f32 v184, v194, v195
	v_cvt_pk_bf16_f32 v185, v156, v197
	s_waitcnt lgkmcnt(0)
	s_barrier
	s_cmp_ge_u32 s33, 2
	s_cbranch_scc0 .Lk_o6
	ds_read2st64_b32 v[116:117], v32 offset0:16 offset1:17
	ds_read2st64_b32 v[118:119], v32 offset0:18 offset1:19
	s_waitcnt lgkmcnt(1)
	v_perm_b32 v132, v117, v116, s58
	s_waitcnt lgkmcnt(0)
	v_perm_b32 v133, v119, v118, s58
	global_store_dwordx2 v33, v[132:133], s[26:27]
	v_perm_b32 v134, v117, v116, s59
	v_perm_b32 v135, v119, v118, s59
	global_store_dwordx2 v34, v[134:135], s[26:27]
	s_add_u32 s26, s26, 0xa0000
	s_addc_u32 s27, s27, 0
.Lk_o6:
	ds_write_b128 v55, v[182:185] offset:13888
	ds_read_b64_tr_b16 v[166:167], v54 offset:13120
	ds_read_b64_tr_b16 v[164:165], v54 offset:13088
	ds_read_b64_tr_b16 v[168:169], v54 offset:13152
	ds_read_b64_tr_b16 v[162:163], v54 offset:13056
	ds_read_b64_tr_b16 v[170:171], v54 offset:13104
	ds_read_b64_tr_b16 v[172:173], v54 offset:13136
	s_waitcnt lgkmcnt(0)
	s_barrier
	s_cmp_eq_u32 s33, 0
	s_cbranch_scc0 .Lk_o8
	ds_read2st64_b32 v[116:117], v32 offset0:32 offset1:33
	ds_read2st64_b32 v[118:119], v32 offset0:34 offset1:35
	s_waitcnt lgkmcnt(1)
	v_perm_b32 v132, v117, v116, s58
	s_waitcnt lgkmcnt(0)
	v_perm_b32 v133, v119, v118, s58
	global_store_dwordx2 v33, v[132:133], s[26:27]
	v_perm_b32 v134, v117, v116, s59
	v_perm_b32 v135, v119, v118, s59
	global_store_dwordx2 v34, v[134:135], s[26:27]
	s_add_u32 s26, s26, 0xa0000
	s_addc_u32 s27, s27, 0
.Lk_o8:
	ds_write_b128 v55, v[166:169] offset:3168
	s_cmp_lt_u32 s33, 2
	s_cbranch_scc0 .Lk_l8
	v_lshlrev_b32 v152, 16, v166
	v_and_b32 v150, s60, v165
	v_and_b32 v153, s60, v166
	v_lshlrev_b32 v149, 16, v165
	v_add_f32 v153, v153, v152
	v_add_f32 v149, v150, v149
	v_lshlrev_b32 v154, 16, v167
	v_and_b32 v148, s60, v164
	v_add_f32 v154, v154, v153
	v_add_f32 v148, v149, v148
	v_and_b32 v155, s60, v167
	v_lshlrev_b32 v147, 16, v164
.Lk_l8:
	s_waitcnt lgkmcnt(0)
	s_barrier
	s_cmp_eq_u32 s33, 1
	s_cbranch_scc0 .Lk_o9
	ds_read2st64_b32 v[116:117], v32 offset0:32 offset1:33
	ds_read2st64_b32 v[118:119], v32 offset0:34 offset1:35
	s_waitcnt lgkmcnt(1)
	v_perm_b32 v132, v117, v116, s58
	s_waitcnt lgkmcnt(0)
	v_perm_b32 v133, v119, v118, s58
	global_store_dwordx2 v33, v[132:133], s[26:27]
	v_perm_b32 v134, v117, v116, s59
	v_perm_b32 v135, v119, v118, s59
	global_store_dwordx2 v34, v[134:135], s[26:27]
	s_add_u32 s26, s26, 0xa0000
	s_addc_u32 s27, s27, 0
.Lk_o9:
	s_cmp_lt_u32 s33, 2
	s_cbranch_scc0 .Lk_l9
	ds_write_b128 v55, v[170:173] offset:7424
.Lk_l9:
	s_cmp_lt_u32 s33, 2
	s_cbranch_scc0 .Lk_l9b
	v_add_f32 v155, v155, v154
	v_add_f32 v147, v148, v147
	v_lshlrev_b32 v156, 16, v168
	v_and_b32 v146, s60, v163
	v_add_f32 v156, v156, v155
	v_add_f32 v146, v147, v146
	v_sub_f32 v190, v146, v149
	v_sub_f32 v191, v147, v150
	v_add_f32 v193, v152, v149
	v_add_f32 v194, v153, v150
	v_sub_f32 v196, v155, v152
	v_sub_f32 v197, v156, v153
	v_cvt_pk_bf16_f32 v178, v190, v191
	v_cvt_pk_bf16_f32 v179, v148, v193
	v_cvt_pk_bf16_f32 v180, v194, v154
	v_cvt_pk_bf16_f32 v181, v196, v197
.Lk_l9b:
	s_waitcnt lgkmcnt(0)
	s_barrier
	s_cmp_eq_u32 s33, 2
	s_cbranch_scc0 .Lk_o10
	ds_read2st64_b32 v[116:117], v32 offset0:32 offset1:33
	ds_read2st64_b32 v[118:119], v32 offset0:34 offset1:35
	s_waitcnt lgkmcnt(1)
	v_perm_b32 v132, v117, v116, s58
	s_waitcnt lgkmcnt(0)
	v_perm_b32 v133, v119, v118, s58
	global_store_dwordx2 v33, v[132:133], s[26:27]
	v_perm_b32 v134, v117, v116, s59
	v_perm_b32 v135, v119, v118, s59
	global_store_dwordx2 v34, v[134:135], s[26:27]
	s_add_u32 s26, s26, 0xa0000
	s_addc_u32 s27, s27, 0
.Lk_o10:
	s_cmp_lt_u32 s33, 2
	s_cbranch_scc0 .Lk_l10
	ds_write_b128 v55, v[178:181] offset:11168
.Lk_l10:
	s_waitcnt lgkmcnt(0)
	s_barrier
	s_cmp_eq_u32 s33, 3
	s_cbranch_scc0 .Lk_o11
	ds_read2st64_b32 v[116:117], v32 offset0:32 offset1:33
	ds_read2st64_b32 v[118:119], v32 offset0:34 offset1:35
	s_waitcnt lgkmcnt(1)
	v_perm_b32 v132, v117, v116, s58
	s_waitcnt lgkmcnt(0)
	v_perm_b32 v133, v119, v118, s58
	global_store_dwordx2 v33, v[132:133], s[26:27]
	v_perm_b32 v134, v117, v116, s59
	v_perm_b32 v135, v119, v118, s59
	global_store_dwordx2 v34, v[134:135], s[26:27]
	s_add_u32 s26, s26, 0xa0000
	s_addc_u32 s27, s27, 0
.Lk_o11:
	s_waitcnt lgkmcnt(0)
	s_barrier
	s_cmp_eq_u32 s33, 0
	s_cbranch_scc0 .Lk_o12
	ds_read2st64_b32 v[116:117], v32 offset0:48 offset1:49
	ds_read2st64_b32 v[118:119], v32 offset0:50 offset1:51
	s_waitcnt lgkmcnt(1)
	v_perm_b32 v132, v117, v116, s58
	s_waitcnt lgkmcnt(0)
	v_perm_b32 v133, v119, v118, s58
	global_store_dwordx2 v33, v[132:133], s[26:27]
	v_perm_b32 v134, v117, v116, s59
	v_perm_b32 v135, v119, v118, s59
	global_store_dwordx2 v34, v[134:135], s[26:27]
	s_add_u32 s26, s26, 0xa0000
	s_addc_u32 s27, s27, 0
.Lk_o12:
	s_waitcnt lgkmcnt(0)
	s_barrier
	s_cmp_eq_u32 s33, 1
	s_cbranch_scc0 .Lk_o13
	ds_read2st64_b32 v[116:117], v32 offset0:48 offset1:49
	ds_read2st64_b32 v[118:119], v32 offset0:50 offset1:51
	s_waitcnt lgkmcnt(1)
	v_perm_b32 v132, v117, v116, s58
	s_waitcnt lgkmcnt(0)
	v_perm_b32 v133, v119, v118, s58
	global_store_dwordx2 v33, v[132:133], s[26:27]
	v_perm_b32 v134, v117, v116, s59
	v_perm_b32 v135, v119, v118, s59
	global_store_dwordx2 v34, v[134:135], s[26:27]
	s_add_u32 s26, s26, 0xa0000
	s_addc_u32 s27, s27, 0
.Lk_o13:
	s_waitcnt lgkmcnt(0)
	s_barrier
	s_cmp_eq_u32 s33, 2
	s_cbranch_scc0 .Lk_o14
	ds_read2st64_b32 v[116:117], v32 offset0:48 offset1:49
	ds_read2st64_b32 v[118:119], v32 offset0:50 offset1:51
	s_waitcnt lgkmcnt(1)
	v_perm_b32 v132, v117, v116, s58
	s_waitcnt lgkmcnt(0)
	v_perm_b32 v133, v119, v118, s58
	global_store_dwordx2 v33, v[132:133], s[26:27]
	v_perm_b32 v134, v117, v116, s59
	v_perm_b32 v135, v119, v118, s59
	global_store_dwordx2 v34, v[134:135], s[26:27]
	s_add_u32 s26, s26, 0xa0000
	s_addc_u32 s27, s27, 0
.Lk_o14:
	s_waitcnt lgkmcnt(0)
	s_barrier
	s_cmp_eq_u32 s33, 3
	s_cbranch_scc0 .Lk_o15
	ds_read2st64_b32 v[116:117], v32 offset0:48 offset1:49
	ds_read2st64_b32 v[118:119], v32 offset0:50 offset1:51
	s_waitcnt lgkmcnt(1)
	v_perm_b32 v132, v117, v116, s58
	s_waitcnt lgkmcnt(0)
	v_perm_b32 v133, v119, v118, s58
	global_store_dwordx2 v33, v[132:133], s[26:27]
	v_perm_b32 v134, v117, v116, s59
	v_perm_b32 v135, v119, v118, s59
	global_store_dwordx2 v34, v[134:135], s[26:27]
	s_add_u32 s26, s26, 0xa0000
	s_addc_u32 s27, s27, 0
.Lk_o15:
	s_endpgm
.Lk_gslow:
	s_add_i32 s54, s54, 1
	s_cmp_gt_u32 s54, 0x30000
	s_cbranch_scc1 .Lk_gok
	s_sleep 1
	s_add_u32 s44, s56, 0x2000
	s_addc_u32 s45, s57, 0
	global_load_dwordx4 v[216:219], v29, s[56:57] offset:0 sc1
	global_load_dwordx4 v[220:223], v29, s[56:57] offset:16 sc1
	global_load_dwordx4 v[224:227], v29, s[44:45] offset:0 sc1
	global_load_dwordx4 v[228:231], v29, s[44:45] offset:16 sc1
	s_waitcnt vmcnt(0)
	s_branch .Lk_gchk
.Lk_fslow:
	s_add_i32 s54, s54, 1
	s_cmp_gt_u32 s54, 0x30000
	s_cbranch_scc1 .Lk_fok
	s_sleep 1
	global_load_dwordx2 v[26:27], v60, s[28:29] sc1
	s_waitcnt vmcnt(0)
	s_branch .Lk_fchk
.Lk_end:
.Lk_help:
	v_readfirstlane_b32 s32, v0
	s_load_dwordx16 s[8:23], s[62:63], 0x40
	s_load_dwordx8 s[24:31], s[62:63], 0x80
	s_lshr_b32 s32, s32, 6
	s_add_i32 s42, s37, -160
	s_cmp_lt_u32 s37, 96
	s_cselect_b32 s33, s37, s42
	s_lshl3_add_u32 s36, s33, s32
	s_min_u32 s36, s36, 639
	s_mul_i32 s42, s36, 384
	v_and_b32 v1, 63, v0
	v_lshl_add_u32 v57, v1, 1, s42
	v_mov_b64 v[150:151], 0
	v_mov_b64 v[156:157], 0
	s_lshl_b32 s43, s36, 1
	v_mov_b32 v58, s43
	v_mov_b32 v59, 0
	v_mov_b32 v60, 0
	s_mul_i32 s61, s33, 24
	s_lshl2_add_u32 s61, s32, s61
	s_cmp_lt_u32 s32, 6
	s_cselect_b32 s61, s61, 0x800
	v_lshrrev_b32 v62, 4, v1
	v_add_u32 v54, s61, v62
	v_min_u32 v62, 0x7ff, v54
	v_mul_u32_u24 v62, 0x500, v62
	v_and_b32 v63, 15, v1
	v_lshlrev_b32 v63, 4, v63
	v_add_u32 v62, v62, v63
	s_cmp_lt_u32 s61, 0x800
	s_waitcnt lgkmcnt(0)
	global_load_short_d16_hi v59, v58, s[16:17]
	global_load_short_d16_hi v150, v57, s[8:9] offset:256
	global_load_short_d16_hi v151, v57, s[10:11] offset:256
	global_load_short_d16_hi v156, v57, s[12:13] offset:256
	global_load_short_d16_hi v157, v57, s[14:15] offset:256
	global_load_dwordx2 v[52:53], v60, s[28:29]
	s_cbranch_scc0 .Lk_hng
	global_load_dwordx4 v[2:5], v62, s[24:25] offset:0
	global_load_dwordx4 v[32:35], v63, s[30:31] offset:0
	global_load_dwordx4 v[6:9], v62, s[24:25] offset:256
	global_load_dwordx4 v[36:39], v63, s[30:31] offset:256
	global_load_dwordx4 v[10:13], v62, s[24:25] offset:512
	global_load_dwordx4 v[40:43], v63, s[30:31] offset:512
	global_load_dwordx4 v[14:17], v62, s[24:25] offset:768
	global_load_dwordx4 v[44:47], v63, s[30:31] offset:768
	global_load_dwordx4 v[18:21], v62, s[24:25] offset:1024
	global_load_dwordx4 v[48:51], v63, s[30:31] offset:1024
.Lk_hng:
	s_mov_b32 s55, 2
	s_mov_b32 s34, 1
	s_mov_b32 s60, 0xffff0000
	s_lshl_b32 s49, s32, 8
	s_add_i32 s50, s49, 45408
	s_cmp_lt_u32 s33, 80
	s_cbranch_scc0 .Lk_hgate
	s_mov_b32 m0, s49
	v_mov_b32 v62, 0x3c00
	ds_write_addtid_b32 v62 offset:43344
	v_and_b32 v62, 31, v1
	v_lshrrev_b32 v63, 5, v1
	v_mul_u32_u24 v62, 2064, v62
	v_lshl_add_u32 v62, v63, 4, v62
	v_add_u32 v30, s49, v62
	s_cmp_lt_u32 s61, 0x800
	s_cbranch_scc1 .Lk_hw
	s_waitcnt vmcnt(1)
.Lk_hw:
	s_setprio 3
	v_mov_b32 v62, 0x3fb8aa3b
	s_waitcnt vmcnt(11)
	v_mul_f32 v63, v59, v62
	v_exp_f32 v63, v63
	v_mul_f32 v150, v62, v150
	v_exp_f32 v150, v150
	v_cvt_pk_bf16_f32 v63, v63, v63
	v_lshlrev_b32 v63, 16, v63
	v_mul_f32 v188, v62, v63
	v_mul_f32 v189, 0x3e22f983, v63
	v_readfirstlane_b32 s40, v63
	v_cvt_pk_bf16_f32 v150, v150, v150
	v_max3_f32 v63, |v156|, |v157|, |v157|
	v_lshlrev_b32 v150, 16, v150
	v_frexp_exp_i32_f32 v63, v63
	v_pk_mul_f32 v[150:151], v[150:151], v[188:189] neg_lo:[1,0]
	v_fract_f32 v151, v151
	v_exp_f32 v150, v150
	v_max_i32_dpp v63, v63, v63 row_shr:1 row_mask:0xf bank_mask:0xf
	v_cos_f32 v182, v151
	v_sin_f32 v183, v151
	v_max_i32_dpp v63, v63, v63 row_shr:2 row_mask:0xf bank_mask:0xf
	v_pk_mul_f32 v[182:183], v[182:183], v[150:151] op_sel_hi:[1,0]
	s_nop 0
	v_max_i32_dpp v63, v63, v63 row_shr:4 row_mask:0xf bank_mask:0xf
	s_nop 1
	v_max_i32_dpp v63, v63, v63 row_shr:8 row_mask:0xf bank_mask:0xf
	s_nop 1
	v_max_i32_dpp v63, v63, v63 row_bcast:15 row_mask:0xa bank_mask:0xf
	s_nop 1
	v_max_i32_dpp v63, v63, v63 row_bcast:31 row_mask:0xc bank_mask:0xf
	s_nop 0
	v_readlane_b32 s42, v63, 63
	s_branch .Lk_task0
.Lk_htail:
	ds_read_b128 v[80:83], v30 offset:0
	ds_read_b128 v[84:87], v30 offset:32
	ds_read_b128 v[88:91], v30 offset:64
	ds_read_b128 v[92:95], v30 offset:96
	ds_read_b128 v[96:99], v30 offset:128
	ds_read_b128 v[100:103], v30 offset:160
	ds_read_b128 v[104:107], v30 offset:192
	ds_read_b128 v[108:111], v30 offset:224
	s_waitcnt lgkmcnt(7)
	v_mfma_f32_32x32x16_f16 v[64:79], v[80:83], v[112:115], 0
	s_waitcnt lgkmcnt(6)
	v_mfma_f32_32x32x16_f16 v[64:79], v[84:87], v[116:119], v[64:79]
	s_waitcnt lgkmcnt(5)
	v_mfma_f32_32x32x16_f16 v[64:79], v[88:91], v[120:123], v[64:79]
	s_waitcnt lgkmcnt(4)
	v_mfma_f32_32x32x16_f16 v[64:79], v[92:95], v[124:127], v[64:79]
	s_waitcnt lgkmcnt(3)
	v_mfma_f32_32x32x16_f16 v[64:79], v[96:99], v[128:131], v[64:79]
	s_waitcnt lgkmcnt(2)
	v_mfma_f32_32x32x16_f16 v[64:79], v[100:103], v[132:135], v[64:79]
	s_waitcnt lgkmcnt(1)
	v_mfma_f32_32x32x16_f16 v[64:79], v[104:107], v[136:139], v[64:79]
	s_waitcnt lgkmcnt(0)
	v_mfma_f32_32x32x16_f16 v[64:79], v[108:111], v[140:143], v[64:79]
	s_mul_i32 s42, s36, 3072
	s_add_i32 s42, s42, 65536
	v_lshl_add_u32 v61, v1, 4, s42
	s_nop 8
	v_mul_f32 v64, s48, v64
	v_mul_f32 v65, s48, v65
	v_mul_f32 v66, s48, v66
	v_mul_f32 v67, s48, v67
	v_mul_f32 v68, s48, v68
	v_mul_f32 v69, s48, v69
	v_mul_f32 v70, s48, v70
	v_mul_f32 v71, s48, v71
	v_mul_f32 v72, s48, v72
	v_mul_f32 v73, s48, v73
	v_mul_f32 v74, s48, v74
	v_mul_f32 v75, s48, v75
	global_store_dwordx4 v61, v[64:67], s[28:29] offset:0 sc1
	global_store_dwordx4 v61, v[68:71], s[28:29] offset:1024 sc1
	global_store_dwordx4 v61, v[72:75], s[28:29] offset:2048 sc1
.Lk_hgate:
	s_cmp_lt_u32 s33, 80
	s_cbranch_scc1 .Lk_hw3
	s_waitcnt vmcnt(0)
.Lk_hw3:
	s_waitcnt vmcnt(3)
	v_readfirstlane_b32 s38, v52
	v_readfirstlane_b32 s39, v53
	s_add_u32 s38, s38, 0x7f4a7c15
	s_addc_u32 s39, s39, 0x9e3779b9
	v_mov_b32 v56, s38
	v_mov_b32 v57, s39
	s_cmp_lt_u32 s61, 0x800
	s_cbranch_scc0 .Lk_hng2
	v_mov_b32 v62, 0
	v_dot2c_f32_bf16 v62, v2, v32
	v_dot2c_f32_bf16 v62, v3, v33
	v_dot2c_f32_bf16 v62, v4, v34
	v_dot2c_f32_bf16 v62, v5, v35
	v_dot2c_f32_bf16 v62, v6, v36
	v_dot2c_f32_bf16 v62, v7, v37
	v_dot2c_f32_bf16 v62, v8, v38
	v_dot2c_f32_bf16 v62, v9, v39
	v_dot2c_f32_bf16 v62, v10, v40
	v_dot2c_f32_bf16 v62, v11, v41
	v_dot2c_f32_bf16 v62, v12, v42
	v_dot2c_f32_bf16 v62, v13, v43
	v_dot2c_f32_bf16 v62, v14, v44
	v_dot2c_f32_bf16 v62, v15, v45
	v_dot2c_f32_bf16 v62, v16, v46
	v_dot2c_f32_bf16 v62, v17, v47
	v_dot2c_f32_bf16 v62, v18, v48
	v_dot2c_f32_bf16 v62, v19, v49
	v_dot2c_f32_bf16 v62, v20, v50
	v_dot2c_f32_bf16 v62, v21, v51
	v_lshrrev_b32 v63, 8, v54
	v_lshlrev_b32 v63, 8, v63
	v_bfe_u32 v61, v54, 4, 2
	v_lshl_add_u32 v63, v61, 6, v63
	v_add_f32_dpp v62, v62, v62 row_ror:8 row_mask:0xf bank_mask:0xf
	v_bfe_u32 v61, v54, 6, 1
	v_lshl_add_u32 v63, v61, 5, v63
	v_add_f32_dpp v62, v62, v62 row_ror:4 row_mask:0xf bank_mask:0xf
	v_and_b32 v61, 7, v54
	v_lshl_add_u32 v63, v61, 2, v63
	v_add_f32_dpp v62, v62, v62 row_ror:2 row_mask:0xf bank_mask:0xf
	v_bfe_u32 v61, v54, 7, 1
	v_lshl_add_u32 v63, v61, 1, v63
	v_add_f32_dpp v62, v62, v62 row_ror:1 row_mask:0xf bank_mask:0xf
	v_bfe_u32 v61, v54, 3, 1
	v_add_u32 v63, v63, v61
	v_cvt_pk_bf16_f32 v62, v62, v62
	v_lshlrev_b32 v62, 16, v62
	v_mul_f32 v62, 0xbfb8aa3b, v62
	v_exp_f32 v62, v62
	v_lshlrev_b32 v63, 3, v63
	v_add_f32 v62, 1.0, v62
	v_rcp_f32 v62, v62
	v_add_u32 v63, 8192, v63
	v_cvt_pk_bf16_f32 v62, v62, v62
	v_mov_b32 v59, v57
	v_bfi_b32 v58, s60, v62, v56
	s_mov_b32 exec_lo, 0x00010001
	s_mov_b32 exec_hi, 0x00010001
	global_store_dwordx2 v63, v[58:59], s[28:29] sc1
	s_mov_b64 exec, -1
.Lk_hng2:
	s_cmp_lt_u32 s33, 80
	s_cbranch_scc0 .Lk_hnf
	s_lshr_b32 s42, s36, 3
	s_lshl_b32 s42, s42, 7
	s_and_b32 s43, s36, 7
	s_lshl3_add_u32 s42, s43, s42
	s_add_i32 s42, s42, 32768
	v_mov_b32 v60, s42
	s_cmp_lt_u32 s61, 0x800
	s_cbranch_scc1 .Lk_hw1
	s_waitcnt vmcnt(0)
.Lk_hw1:
	s_waitcnt vmcnt(1)
	s_mov_b64 exec, 1
	global_store_dwordx2 v60, v[56:57], s[28:29] sc1
	s_mov_b64 exec, -1
.Lk_hnf:
	s_barrier
	s_cmp_eq_u32 s32, 7
	s_cbranch_scc0 .Lk_hend
	s_lshl_b32 s42, s37, 3
	s_add_i32 s42, s42, 4096
	v_mov_b32 v60, s42
	s_mov_b64 exec, 1
	global_store_dwordx2 v60, v[56:57], s[28:29] sc1
	s_mov_b64 exec, -1
	s_cmp_eq_u32 s33, 95
	s_cbranch_scc0 .Lk_hend
	v_lshlrev_b32 v60, 3, v1
	v_add_u32 v60, 4096, v60
	s_mov_b32 s54, 0
.Lk_hpoll:
	s_sleep 16
	s_add_i32 s54, s54, 1
	s_cmp_gt_u32 s54, 0x30000
	s_cbranch_scc1 .Lk_hend
	global_load_dwordx2 v[2:3], v60, s[28:29] offset:0 sc1
	global_load_dwordx2 v[4:5], v60, s[28:29] offset:512 sc1
	global_load_dwordx2 v[6:7], v60, s[28:29] offset:1024 sc1
	global_load_dwordx2 v[8:9], v60, s[28:29] offset:1536 sc1
	s_waitcnt vmcnt(0)
	v_xor_b32 v62, v2, v56
	v_bitop3_b32 v62, v3, v57, v62 bitop3:0xbe
	v_bitop3_b32 v62, v4, v56, v62 bitop3:0xbe
	v_bitop3_b32 v62, v5, v57, v62 bitop3:0xbe
	v_bitop3_b32 v62, v6, v56, v62 bitop3:0xbe
	v_bitop3_b32 v62, v7, v57, v62 bitop3:0xbe
	v_bitop3_b32 v62, v8, v56, v62 bitop3:0xbe
	v_bitop3_b32 v62, v9, v57, v62 bitop3:0xbe
	v_cmp_ne_u32 vcc, 0, v62
	s_cbranch_vccnz .Lk_hpoll
	v_mov_b32 v60, 0
	s_mov_b64 exec, 1
	global_store_dwordx2 v60, v[56:57], s[28:29] sc1
.Lk_hend:
	s_endpgm
)ASM" ::"s"(__builtin_amdgcn_kernarg_segment_ptr()), "s"(blockIdx.x), "v"(tid), "s"((const GAS u32*)lane_tab)
               : "memory", "vcc", "scc", "s4", "s5", "v1", "v2", "v3", "v4", "v5", "v6", "v7", "v8", "v9", "v10", "v11", "v12", "v13", "v14", "v15", "v16", "v17", "v18", "v19", "v20", "v21", "v22", "v23", "v24", "v25", "v26", "v27", "v28", "v29", "v30", "v31", "v32", "v33", "v34", "v35", "v36", "v37", "v38", "v39", "v40", "v41", "v42", "v43", "v44", "v45", "v46", "v47", "v48", "v49", "v50", "v51", "v52", "v53", "v54", "v55", "v56", "v57", "v58", "v59", "v60", "v61", "v62", "v63", "v64", "v65", "v66", "v67", "v68", "v69", "v70", "v71", "v72", "v73", "v74", "v75", "v76", "v77", "v78", "v79", "v80", "v81", "v82", "v83", "v84", "v85", "v86", "v87", "v88", "v89", "v90", "v91", "v92", "v93", "v94", "v95", "v96", "v97", "v98", "v99", "v100", "v101", "v102", "v103", "v104", "v105", "v106", "v107", "v108", "v109", "v110", "v111", "v112", "v113", "v114", "v115", "v116", "v117", "v118", "v119", "v120", "v121", "v122", "v123", "v124", "v125", "v126", "v127", "v128", "v129", "v130", "v131", "v132", "v133", "v134", "v135", "v136", "v137", "v138", "v139", "v140", "v141", "v142", "v143", "v144", "v145", "v146", "v147", "v148", "v149", "v150", "v151", "v152", "v153", "v154", "v155", "v156", "v157", "v158", "v159", "v160", "v161", "v162", "v163", "v164", "v165", "v166", "v167", "v168", "v169", "v170", "v171", "v172", "v173", "v174", "v175", "v176", "v177", "v178", "v179", "v180", "v181", "v182", "v183", "v184", "v185", "v186", "v187", "v188", "v189", "v190", "v191", "v192", "v193", "v194", "v195", "v196", "v197", "v198", "v199", "v200", "v201", "v202", "v203", "v204", "v205", "v206", "v207", "v208", "v209", "v210", "v211", "v212", "v213", "v214", "v215", "v216", "v217", "v218", "v219", "v220", "v221", "v222", "v223", "v224", "v225", "v226", "v227", "v228", "v229", "v230", "v231", "v232", "v233", "v234", "v235", "v236", "v237", "v238", "v239", "v240", "v241", "v242", "v243", "v244", "v245", "v246", "v247", "v248", "v249", "v250", "v251", "v252", "v253", "v254", "v255", "s8", "s9", "s10", "s11", "s12", "s13", "s14", "s15", "s16", "s17", "s18", "s19", "s20", "s21", "s22", "s23", "s24", "s25", "s26", "s27", "s28", "s29", "s30", "s31", "s32", "s33", "s34", "s35", "s36", "s37", "s38", "s39", "s40", "s41", "s42", "s43", "s44", "s45", "s46", "s47", "s48", "s49", "s50", "s51", "s52", "s53", "s54", "s55", "s56", "s57", "s58", "s59", "s60", "s61", "s62", "s63", "s64", "s65", "s66", "s67", "s68", "s69", "s70", "s71", "s72", "s73", "s74", "s75", "s76", "s77", "s78", "s79", "s80", "s81", "s82", "s83", "s84", "s85", "s86", "s87", "s88", "s89", "s90", "s91", "s92", "s93", "s94", "s95");
}

extern "C" void kernel_launch(void* const* d_in, const int* in_sizes, int n_in,
                              void* d_out, int out_size, void* d_ws, size_t ws_size,
                              hipStream_t stream) {
  const u16* const* in = (const u16* const*)d_in;
  (void)hipFuncSetAttribute((const void*)ssm_mix, hipFuncAttributeMaxDynamicSharedMemorySize, LDS_BYTES);
  ssm_mix<<<256, 512, LDS_BYTES, stream>>>(in[1], in[2], in[5], in[6], in[7], in[8], in[3], in[4],
                                           in[10], in[11], in[12], in[13], in[14], in[15], in[12], in[13],
                                           in[0], (u16*)d_out, (char*)d_ws, in[16]);
}
```
